```python
import torch

def _softmax(x, axis=-1):
    return torch.softmax(x, dim=axis)
def _gelu(x):
    return torch.nn.functional.gelu(x, approximate="tanh")
def _silu(x):
    return torch.nn.functional.silu(x)
def _sigmoid(x):
    return torch.sigmoid(x)
def _layernorm(x, axis=-1, eps=1e-5):
    m = x.mean(dim=axis, keepdim=True)
    v = x.var(dim=axis, keepdim=True, unbiased=False)
    return (x - m) / torch.sqrt(v + eps)
def _scatter_add(tgt, idx, src, axis=0):
    return tgt.clone().index_add_(axis, idx, src)
def _segment_sum(src, seg, n):
    out = torch.zeros((n,) + src.shape[1:], dtype=src.dtype)
    return out.index_add_(0, seg, src)
def _pad(x, pw):
    flat = tuple(v for pair in reversed(pw) for v in pair)
    return torch.nn.functional.pad(x, flat)

kernel_name = 'standalone__gqa_block_cached__near_ridge'

def setup_inputs():

    _w = (lambda a: (lambda t: t.to(torch.bfloat16) if t.is_floating_point() else t)(torch.as_tensor(a)))
    B, Sq, D = 32, 32, 512
    Hq, Hkv, Sk, Dh = 32, 8, 512, 128
    assert Hq % Hkv == 0, (Hq, Hkv)
    return {
        "x": _w((lambda *s: torch.randn(*s, dtype=torch.bfloat16) * 0.8)(B, Sq, D)),
        "k_cache": _w((lambda *s: torch.randn(*s, dtype=torch.bfloat16) * 0.8)(B, Hkv, Sk, Dh)),
        "v_cache": _w((lambda *s: torch.randn(*s, dtype=torch.bfloat16) * 0.05)(B, Hkv, Sk, Dh)),
        "wq": _w((lambda *s: torch.randn(*s, dtype=torch.bfloat16) * 0.8)(D, Hq * Dh) * (D ** -0.5)),
        "wo": _w((lambda *s: torch.randn(*s, dtype=torch.bfloat16) * 0.05)(Hq * Dh, D) * ((Hq * Dh) ** -0.5)),
    }

def reference(x, k_cache, v_cache, wq, wo):
    B, Sq, D = x.shape
    Hq, Dh = 32, 128
    Hkv = k_cache.shape[1]
    rep = Hq // Hkv
    q = (lambda x, *d: x.permute(*d))((x @ wq).reshape(B, Sq, Hq, Dh), 0, 2, 1, 3)
    k = (lambda x, n, axis: torch.repeat_interleave(x, n, dim=axis))(k_cache, rep, axis=1)
    v = (lambda x, n, axis: torch.repeat_interleave(x, n, dim=axis))(v_cache, rep, axis=1)
    scores = torch.einsum("bhqd,bhkd->bhqk", q, k) * (Dh ** -0.5)
    p = _softmax(scores, axis=-1)
    o = torch.einsum("bhqk,bhkd->bhqd", p, v)
    return (lambda x, *d: x.permute(*d))(o, 0, 2, 1, 3).reshape(B, Sq, Hq * Dh) @ wo
```

```cpp
#include <hip/hip_runtime.h>
#include <hip/hip_bf16.h>

typedef __attribute__((ext_vector_type(8))) short bf16x8;
typedef __attribute__((ext_vector_type(4))) short s16x4;
typedef __attribute__((ext_vector_type(4))) float f32x4;
typedef __attribute__((ext_vector_type(16))) float f32x16;
typedef __attribute__((ext_vector_type(4))) unsigned u32x4;
typedef __attribute__((ext_vector_type(2))) unsigned u32x2;
typedef unsigned short u16;

#define DI __device__ __forceinline__
#define LDS3 __attribute__((address_space(3)))

constexpr int B_ = 32, SQ = 32, DM = 512, HQ = 32, HKV = 8, SK = 512, DH = 128;
constexpr int LDS_RING = 0;
constexpr int LDS_X = 131072;
constexpr int LDS_TOTAL = 163840;

#define STAMP(i) do {} while (0)

typedef __attribute__((ext_vector_type(2))) __bf16 bf16x2_t;
typedef __attribute__((ext_vector_type(2))) float f32x2;
DI unsigned cvtpk(float lo, float hi) {
  f32x2 v = {lo, hi};
  return __builtin_bit_cast(unsigned, __builtin_convertvector(v, bf16x2_t));
}

DI void barrier_lgkm() { asm volatile("s_waitcnt lgkmcnt(0)\n\ts_barrier" ::: "memory"); }

DI void glds16(const void* gsrc, unsigned lds_dst) {
  unsigned keep;
  asm volatile("s_mov_b32 %0, m0\n\ts_mov_b32 m0, %2\n\ts_nop 0\n\tglobal_load_lds_dwordx4 %1, off\n\ts_mov_b32 m0, %0"
               : "=&s"(keep) : "v"(gsrc), "s"(lds_dst) : "memory");
}

template <int RS>
DI void wload(u32x4 (&raw)[8], const u16* p, int s) {
#pragma unroll
  for (int j = 0; j < 8; ++j) raw[j] = *(const u32x4*)(p + (size_t)(32 * s + j) * RS);
}
DI void wstep(const u32x4 (&raw)[8], f32x4 (&acc)[8][2], const char* bimg, int s, const unsigned (&boff)[4]) {
  const bf16x8 b0 = *(const bf16x8*)(bimg + boff[s & 3] + 256 * (s >> 2));
  const bf16x8 b1 = *(const bf16x8*)(bimg + boff[s & 3] + 256 * (s >> 2) + 16384);
#pragma unroll
  for (int c = 0; c < 4; ++c) {
    u32x4 f0, f1;
#pragma unroll
    for (int m = 0; m < 4; ++m) {
      f0[m] = __builtin_amdgcn_perm(raw[2 * m + 1][c], raw[2 * m][c], 0x05040100u);
      f1[m] = __builtin_amdgcn_perm(raw[2 * m + 1][c], raw[2 * m][c], 0x07060302u);
    }
    const bf16x8 a0 = __builtin_bit_cast(bf16x8, f0), a1 = __builtin_bit_cast(bf16x8, f1);
    acc[2 * c][0] = __builtin_amdgcn_mfma_f32_16x16x32_bf16(a0, b0, acc[2 * c][0], 0, 0, 0);
    acc[2 * c][1] = __builtin_amdgcn_mfma_f32_16x16x32_bf16(a0, b1, acc[2 * c][1], 0, 0, 0);
    acc[2 * c + 1][0] = __builtin_amdgcn_mfma_f32_16x16x32_bf16(a1, b0, acc[2 * c + 1][0], 0, 0, 0);
    acc[2 * c + 1][1] = __builtin_amdgcn_mfma_f32_16x16x32_bf16(a1, b1, acc[2 * c + 1][1], 0, 0, 0);
  }
}

__global__ __launch_bounds__(512, 2) void fused_kernel(const u16* x, const u16* kc, const u16* vc, const u16* wq, const u16* wo,
                                                       float* part
) {
  extern __shared__ __attribute__((aligned(16))) char smem[];
  const int tid = threadIdx.x, lane = tid & 63;
  const int wave = __builtin_amdgcn_readfirstlane(tid >> 6);
  const int hkv = blockIdx.x & 7, b = blockIdx.x >> 3;
  const int w4 = wave & 3;
  const int r16 = lane & 15, g = lane >> 4;
  STAMP(0);
  unsigned boff[4];
#pragma unroll
  for (int i = 0; i < 4; ++i) boff[i] = r16 * 1024 + 16 * ((4 * i + g) ^ r16);
  const char* ximg = smem + LDS_X;

  if (wave < 4) {
    const u16* wp = wq + (size_t)(8 * g) * 4096 + hkv * 512 + w4 * 128 + r16 * 8;
    u32x4 raw[3][8];
    wload<4096>(raw[0], wp, 0);
    wload<4096>(raw[1], wp, 1);
    wload<4096>(raw[2], wp, 2);
    f32x4 acc[8][2];
#pragma unroll
    for (int t = 0; t < 8; ++t) { acc[t][0] = f32x4{0, 0, 0, 0}; acc[t][1] = f32x4{0, 0, 0, 0}; }
    barrier_lgkm();
    STAMP(1);
#pragma unroll
    for (int s = 0; s < 16; ++s) {
      wstep(raw[s % 3], acc, ximg, s, boff);
      if (s + 3 < 16) wload<4096>(raw[s % 3], wp, s + 3);
    }
    bf16x8 qf[8];
#pragma unroll
    for (int i = 0; i < 4; ++i) {
      u32x4 a, bb;
#pragma unroll
      for (int m = 0; m < 4; ++m) {
        unsigned a_ = cvtpk(acc[2 * m][0][i], acc[2 * m + 1][0][i]);
        unsigned b_ = cvtpk(acc[2 * m][1][i], acc[2 * m + 1][1][i]);
        auto r = __builtin_amdgcn_permlane16_swap(a_, b_, false, false);
        a[m] = r[0]; bb[m] = r[1];
      }
      qf[i] = __builtin_bit_cast(bf16x8, a);
      qf[4 + i] = __builtin_bit_cast(bf16x8, bb);
    }
    STAMP(2);
    const int r32 = lane & 31, h = lane >> 5;
    unsigned kaddr[8], vaddr[8];
#pragma unroll
    for (int s = 0; s < 8; ++s) kaddr[s] = r32 * 256 + 16 * ((8 * h + s) ^ (r32 & 15));
    {
      const int blk = (lane >> 4) & 1, qq = (lane & 15) >> 2, p = lane & 3;
#pragma unroll
      for (int dt = 0; dt < 4; ++dt)
#pragma unroll
        for (int e = 0; e < 2; ++e)
          vaddr[dt * 2 + e] = (8 * e + 4 * h + qq) * 256 + 16 * (4 * (dt ^ qq) + ((2 * blk + (p >> 1)) ^ (2 * e + h))) + 8 * (p & 1) + 16384;
    }
    const float csc = 0.08838834764831845f * 1.4426950408889634f;
    float m_run = -__builtin_inff(), l_run = 0.f;
    f32x16 o[4];
#pragma unroll
    for (int dt = 0; dt < 4; ++dt)
#pragma unroll
      for (int i = 0; i < 16; ++i) o[dt][i] = 0.f;

    for (int t = 0; t < 8; ++t) {
      barrier_lgkm();
      const char* slot = smem + LDS_RING + (t & 3) * 32768;
      f32x16 s0, s1;
#pragma unroll
      for (int i = 0; i < 16; ++i) { s0[i] = 0.f; s1[i] = 0.f; }
#pragma unroll
      for (int s = 0; s < 8; ++s) {
        const bf16x8 k0 = *(const bf16x8*)(slot + kaddr[s]);
        const bf16x8 k1 = *(const bf16x8*)(slot + kaddr[s] + 8192);
        s0 = __builtin_amdgcn_mfma_f32_32x32x16_bf16(k0, qf[s], s0, 0, 0, 0);
        s1 = __builtin_amdgcn_mfma_f32_32x32x16_bf16(k1, qf[s], s1, 0, 0, 0);
      }
      float mx = s0[0];
#pragma unroll
      for (int i = 1; i < 16; ++i) mx = fmaxf(mx, s0[i]);
#pragma unroll
      for (int i = 0; i < 16; ++i) mx = fmaxf(mx, s1[i]);
      {
        auto rr = __builtin_amdgcn_permlane32_swap(__float_as_uint(mx), __float_as_uint(mx), false, false);
        mx = fmaxf(__uint_as_float(rr[0]), __uint_as_float(rr[1]));
      }
      const float m_new = fmaxf(m_run, mx);
      const float alpha = __builtin_amdgcn_exp2f((m_run - m_new) * csc);
      const float nm = -m_new * csc;
      m_run = m_new;
      float ps = 0.f;
#pragma unroll
      for (int i = 0; i < 16; ++i) { s0[i] = __builtin_amdgcn_exp2f(fmaf(s0[i], csc, nm)); ps += s0[i]; }
#pragma unroll
      for (int i = 0; i < 16; ++i) { s1[i] = __builtin_amdgcn_exp2f(fmaf(s1[i], csc, nm)); ps += s1[i]; }
      l_run = l_run * alpha + ps;
      bf16x8 pf[4];
#pragma unroll
      for (int s2 = 0; s2 < 2; ++s2) {
        u32x4 a, c;
#pragma unroll
        for (int m = 0; m < 4; ++m) {
          a[m] = cvtpk(s0[8 * s2 + 2 * m], s0[8 * s2 + 2 * m + 1]);
          c[m] = cvtpk(s1[8 * s2 + 2 * m], s1[8 * s2 + 2 * m + 1]);
        }
        pf[s2] = __builtin_bit_cast(bf16x8, a);
        pf[2 + s2] = __builtin_bit_cast(bf16x8, c);
      }
#pragma unroll
      for (int dt = 0; dt < 4; ++dt)
#pragma unroll
        for (int i = 0; i < 16; ++i) o[dt][i] *= alpha;
#pragma unroll
      for (int dt = 0; dt < 4; ++dt) {
#pragma unroll
        for (int kk = 0; kk < 4; ++kk) {
          const s16x4 lo = __builtin_amdgcn_ds_read_tr16_b64_v4i16((LDS3 s16x4*)(slot + vaddr[dt * 2 + 0] + 4096 * kk));
          const s16x4 hi = __builtin_amdgcn_ds_read_tr16_b64_v4i16((LDS3 s16x4*)(slot + vaddr[dt * 2 + 1] + 4096 * kk));
          const bf16x8 vf = __builtin_shufflevector(lo, hi, 0, 1, 2, 3, 4, 5, 6, 7);
          o[dt] = __builtin_amdgcn_mfma_f32_32x32x16_bf16(vf, pf[kk], o[dt], 0, 0, 0);
        }
      }
    }
    STAMP(3);
    {
      auto rr = __builtin_amdgcn_permlane32_swap(__float_as_uint(l_run), __float_as_uint(l_run), false, false);
      l_run = __uint_as_float(rr[0]) + __uint_as_float(rr[1]);
    }
    const float inv = 1.0f / l_run;
#pragma unroll
    for (int dt = 0; dt < 4; ++dt)
#pragma unroll
      for (int a = 0; a < 4; ++a) {
        u32x2 v;
        v[0] = cvtpk(o[dt][4 * a] * inv, o[dt][4 * a + 1] * inv);
        v[1] = cvtpk(o[dt][4 * a + 2] * inv, o[dt][4 * a + 3] * inv);
        const int c = 16 * w4 + 4 * dt + a;
        *(u32x2*)(smem + LDS_X + r32 * 1024 + 16 * (c ^ (r32 & 15)) + 8 * h) = v;
      }
    barrier_lgkm();
    STAMP(4);
  } else {
    const unsigned sbase = (unsigned)(size_t)smem;
    {
      const u16* xp = x + (size_t)b * SQ * DM;
#pragma unroll
      for (int i = 0; i < 8; ++i) {
        const int row = 8 * w4 + i;
        const int c = (lane & ~15) | ((lane & 15) ^ (row & 15));
        glds16(xp + row * DM + c * 8, sbase + LDS_X + row * 1024);
      }
    }
    const u16* kp = kc + (size_t)(b * HKV + hkv) * SK * DH;
    const u16* vp = vc + (size_t)(b * HKV + hkv) * SK * DH;
    unsigned koff[4], voff[4];
#pragma unroll
    for (int j = 0; j < 4; ++j) {
      const int row = 16 * w4 + 4 * j + g;
      koff[j] = row * DH + 8 * (r16 ^ (row & 15));
      voff[j] = row * DH + 8 * (r16 ^ (((row & 3) << 2) | ((row >> 2) & 3)));
    }
#pragma unroll
    for (int t = 0; t < 4; ++t) {
#pragma unroll
      for (int j = 0; j < 4; ++j) glds16(kp + t * 64 * DH + koff[j], sbase + LDS_RING + t * 32768 + (16 * w4 + 4 * j) * 256);
#pragma unroll
      for (int j = 0; j < 4; ++j) glds16(vp + t * 64 * DH + voff[j], sbase + LDS_RING + t * 32768 + 16384 + (16 * w4 + 4 * j) * 256);
    }
    asm volatile("s_waitcnt vmcnt(32)" ::: "memory");
    barrier_lgkm();
    STAMP(1);
    u32x4 kv[4][8];
#pragma unroll
    for (int t = 0; t < 4; ++t) {
#pragma unroll
      for (int j = 0; j < 4; ++j) kv[t][j] = *(const u32x4*)(kp + (t + 4) * 64 * DH + koff[j]);
#pragma unroll
      for (int j = 0; j < 4; ++j) kv[t][4 + j] = *(const u32x4*)(vp + (t + 4) * 64 * DH + voff[j]);
    }
    asm volatile("s_waitcnt vmcnt(56)" ::: "memory");
    barrier_lgkm();
    asm volatile("s_waitcnt vmcnt(48)" ::: "memory");
    barrier_lgkm();
    asm volatile("s_waitcnt vmcnt(40)" ::: "memory");
    barrier_lgkm();
    asm volatile("s_waitcnt vmcnt(32)" ::: "memory");
    barrier_lgkm();
    const u16* wp = wo + (size_t)(hkv * 512 + 8 * g) * 512 + w4 * 128 + r16 * 8;
    u32x4 raw[4][8];
    wload<512>(raw[0], wp, 0);
    wload<512>(raw[1], wp, 1);
#pragma unroll
    for (int t = 0; t < 4; ++t) {
      char* dst = smem + LDS_RING + t * 32768 + 16 * w4 * 256 + lane * 16;
#pragma unroll
      for (int j = 0; j < 4; ++j) {
        *(u32x4*)(dst + j * 1024) = kv[t][j];
        *(u32x4*)(dst + 16384 + j * 1024) = kv[t][4 + j];
      }
      if (t == 0) wload<512>(raw[2], wp, 2);
      if (t == 1) wload<512>(raw[3], wp, 3);
      barrier_lgkm();
    }
    f32x4 acc[8][2];
#pragma unroll
    for (int t = 0; t < 8; ++t) { acc[t][0] = f32x4{0, 0, 0, 0}; acc[t][1] = f32x4{0, 0, 0, 0}; }
    STAMP(3);
    barrier_lgkm();
    STAMP(4);
#pragma unroll
    for (int s = 0; s < 16; ++s) {
      wstep(raw[s & 3], acc, ximg, s, boff);
      if (s + 4 < 16) wload<512>(raw[s & 3], wp, s + 4);
    }
    STAMP(5);
    float* pp = part + (size_t)blockIdx.x * (SQ * DM) + 128 * w4 + 32 * g;
#pragma unroll
    for (int qt = 0; qt < 2; ++qt)
#pragma unroll
      for (int i = 0; i < 4; ++i) {
        f32x4 v0 = {acc[0][qt][i], acc[1][qt][i], acc[2][qt][i], acc[3][qt][i]};
        f32x4 v1 = {acc[4][qt][i], acc[5][qt][i], acc[6][qt][i], acc[7][qt][i]};
        float* d = pp + (size_t)(r16 + 16 * qt) * DM + 8 * i;
        *(f32x4*)d = v0;
        *(f32x4*)(d + 4) = v1;
      }
    STAMP(6);
  }
}

__global__ __launch_bounds__(256) void reduce_kernel(const float* part, u16* out) {
  const int idx = blockIdx.x * 256 + threadIdx.x;
  const int b = idx >> 12, rem = (idx & 4095) * 4;
  const float* p = part + (size_t)b * 8 * (SQ * DM) + rem;
  f32x4 s = *(const f32x4*)p;
#pragma unroll
  for (int h = 1; h < 8; ++h) s += *(const f32x4*)(p + (size_t)h * (SQ * DM));
  u32x2 v;
  v[0] = cvtpk(s[0], s[1]);
  v[1] = cvtpk(s[2], s[3]);
  *(u32x2*)(out + (size_t)b * (SQ * DM) + rem) = v;
}

extern "C" void kernel_launch(void* const* d_in, const int* in_sizes, int n_in, void* d_out, int out_size, void* d_ws,
                              size_t ws_size, hipStream_t stream) {
  const u16* x = (const u16*)d_in[0];
  const u16* kc = (const u16*)d_in[1];
  const u16* vc = (const u16*)d_in[2];
  const u16* wq = (const u16*)d_in[3];
  const u16* wo = (const u16*)d_in[4];
  float* part = (float*)d_ws;
  (void)hipFuncSetAttribute((const void*)fused_kernel, hipFuncAttributeMaxDynamicSharedMemorySize, LDS_TOTAL);
  fused_kernel<<<256, 512, LDS_TOTAL, stream>>>(x, kc, vc, wq, wo, part);
  reduce_kernel<<<512, 256, 0, stream>>>(part, (u16*)d_out);
}
```

```cpp
#include <hip/hip_runtime.h>
#include <hip/hip_bf16.h>

typedef __attribute__((ext_vector_type(8))) short bf16x8;
typedef __attribute__((ext_vector_type(4))) short s16x4;
typedef __attribute__((ext_vector_type(4))) float f32x4;
typedef __attribute__((ext_vector_type(16))) float f32x16;
typedef __attribute__((ext_vector_type(4))) unsigned u32x4;
typedef __attribute__((ext_vector_type(2))) unsigned u32x2;
typedef unsigned short u16;

#define DI __device__ __forceinline__
#define LDS3 __attribute__((address_space(3)))

constexpr int B_ = 32, SQ = 32, DM = 512, HQ = 32, HKV = 8, SK = 512, DH = 128;
constexpr int LDS_RING = 0;
constexpr int LDS_X = 131072;
constexpr int LDS_TOTAL = 163840;

#define STAMP(i) do {} while (0)
#define STAMP_DECL
#define STAMP_FLUSH do {} while (0)

typedef __attribute__((ext_vector_type(2))) __bf16 bf16x2_t;
typedef __attribute__((ext_vector_type(2))) float f32x2;
DI unsigned cvtpk(float lo, float hi) {
  f32x2 v = {lo, hi};
  return __builtin_bit_cast(unsigned, __builtin_convertvector(v, bf16x2_t));
}

DI void barrier_lgkm() { asm volatile("s_waitcnt lgkmcnt(0)\n\ts_barrier" ::: "memory"); }

DI void glds16(const void* sbase, unsigned voff, unsigned lds_dst) {
  unsigned keep;
  asm volatile("s_mov_b32 %0, m0\n\ts_mov_b32 m0, %3\n\ts_nop 0\n\tglobal_load_lds_dwordx4 %1, %2\n\ts_mov_b32 m0, %0"
               : "=&s"(keep) : "v"(voff), "s"(sbase), "s"(lds_dst) : "memory");
}
DI void glds16_nt(const void* sbase, unsigned voff, unsigned lds_dst) {
  unsigned keep;
  asm volatile("s_mov_b32 %0, m0\n\ts_mov_b32 m0, %3\n\ts_nop 0\n\tglobal_load_lds_dwordx4 %1, %2 nt\n\ts_mov_b32 m0, %0"
               : "=&s"(keep) : "v"(voff), "s"(sbase), "s"(lds_dst) : "memory");
}
DI void gload16(u32x4& dst, const void* sbase, unsigned voff) {
  asm volatile("global_load_dwordx4 %0, %1, %2" : "=v"(dst) : "v"(voff), "s"(sbase) : "memory");
}
template <int N>
DI void wait_raw(u32x4 (&r)[8]) {
  asm volatile("s_waitcnt vmcnt(%8)"
               : "+v"(r[0]), "+v"(r[1]), "+v"(r[2]), "+v"(r[3]), "+v"(r[4]), "+v"(r[5]), "+v"(r[6]), "+v"(r[7]) : "i"(N) : "memory");
}
template <int N> DI void wait_vm() { asm volatile("s_waitcnt vmcnt(%0)" :: "i"(N) : "memory"); }

template <int RS>
DI void wload(u32x4 (&raw)[8], const u16* p, unsigned voff, int s) {
#pragma unroll
  for (int j = 0; j < 8; ++j) gload16(raw[j], p + (size_t)(32 * s + j) * RS, voff);
}
DI void wstep(const u32x4 (&raw)[8], f32x4 (&acc)[8][2], const char* bimg, int s, const unsigned (&boff)[4]) {
  const bf16x8 b0 = *(const bf16x8*)(bimg + boff[s & 3] + 256 * (s >> 2));
  const bf16x8 b1 = *(const bf16x8*)(bimg + boff[s & 3] + 256 * (s >> 2) + 16384);
#pragma unroll
  for (int c = 0; c < 4; ++c) {
    u32x4 f0, f1;
#pragma unroll
    for (int m = 0; m < 4; ++m) {
      f0[m] = __builtin_amdgcn_perm(raw[2 * m + 1][c], raw[2 * m][c], 0x05040100u);
      f1[m] = __builtin_amdgcn_perm(raw[2 * m + 1][c], raw[2 * m][c], 0x07060302u);
    }
    const bf16x8 a0 = __builtin_bit_cast(bf16x8, f0), a1 = __builtin_bit_cast(bf16x8, f1);
    acc[2 * c][0] = __builtin_amdgcn_mfma_f32_16x16x32_bf16(a0, b0, acc[2 * c][0], 0, 0, 0);
    acc[2 * c][1] = __builtin_amdgcn_mfma_f32_16x16x32_bf16(a0, b1, acc[2 * c][1], 0, 0, 0);
    acc[2 * c + 1][0] = __builtin_amdgcn_mfma_f32_16x16x32_bf16(a1, b0, acc[2 * c + 1][0], 0, 0, 0);
    acc[2 * c + 1][1] = __builtin_amdgcn_mfma_f32_16x16x32_bf16(a1, b1, acc[2 * c + 1][1], 0, 0, 0);
  }
}

__global__ __launch_bounds__(512, 2) void fused_kernel(const u16* x, const u16* kc, const u16* vc, const u16* wq, const u16* wo,
                                                       float* part
) {
  extern __shared__ __attribute__((aligned(16))) char smem[];
  const int tid = threadIdx.x, lane = tid & 63;
  const int wave = __builtin_amdgcn_readfirstlane(tid >> 6);
  const int hkv = blockIdx.x & 7, b = blockIdx.x >> 3;
  const int w4 = wave & 3;
  const int r16 = lane & 15, g = lane >> 4;
  STAMP_DECL
  STAMP(0);
  unsigned boff[4];
#pragma unroll
  for (int i = 0; i < 4; ++i) boff[i] = r16 * 1024 + 16 * ((4 * i + g) ^ r16);
  const char* ximg = smem + LDS_X;
  const int stag = b & 3;

  if (wave < 4) {
    const u16* wp = wq + hkv * 512 + w4 * 128;
    const unsigned wv = (unsigned)(8 * g * 4096 + r16 * 8) * 2;
    u32x4 raw[4][8];
    wload<4096>(raw[0], wp, wv, 0);
    wload<4096>(raw[1], wp, wv, 1);
    wload<4096>(raw[2], wp, wv, 2);
    wload<4096>(raw[3], wp, wv, 3);
    f32x4 acc[8][2];
#pragma unroll
    for (int t = 0; t < 8; ++t) { acc[t][0] = f32x4{0, 0, 0, 0}; acc[t][1] = f32x4{0, 0, 0, 0}; }
    barrier_lgkm();
    STAMP(1);
#pragma unroll
    for (int s = 0; s < 16; ++s) {
      if (s < 13) wait_raw<24>(raw[s & 3]); else if (s == 13) wait_raw<16>(raw[s & 3]); else if (s == 14) wait_raw<8>(raw[s & 3]); else wait_raw<0>(raw[s & 3]);
      wstep(raw[s & 3], acc, ximg, s, boff);
      __builtin_amdgcn_sched_barrier(0);
      if (s + 4 < 16) wload<4096>(raw[s & 3], wp, wv, s + 4);
      if (s >= 1 && s <= 12 && ((s - 1) & 3) == stag) asm volatile("s_barrier" ::: "memory");
    }
    bf16x8 qf[8];
#pragma unroll
    for (int i = 0; i < 4; ++i) {
      u32x4 a, bb;
#pragma unroll
      for (int m = 0; m < 4; ++m) {
        unsigned a_ = cvtpk(acc[2 * m][0][i], acc[2 * m + 1][0][i]);
        unsigned b_ = cvtpk(acc[2 * m][1][i], acc[2 * m + 1][1][i]);
        auto r = __builtin_amdgcn_permlane16_swap(a_, b_, false, false);
        a[m] = r[0]; bb[m] = r[1];
      }
      qf[i] = __builtin_bit_cast(bf16x8, a);
      qf[4 + i] = __builtin_bit_cast(bf16x8, bb);
    }
    STAMP(2);
    const int r32 = lane & 31, h = lane >> 5;
    unsigned kaddr[8], vaddr[8];
#pragma unroll
    for (int s = 0; s < 8; ++s) kaddr[s] = r32 * 256 + 16 * ((8 * h + s) ^ (r32 & 15));
    {
      const int blk = (lane >> 4) & 1, qq = (lane & 15) >> 2, p = lane & 3;
#pragma unroll
      for (int dt = 0; dt < 4; ++dt)
#pragma unroll
        for (int e = 0; e < 2; ++e)
          vaddr[dt * 2 + e] = (8 * e + 4 * h + qq) * 256 + 16 * (4 * (dt ^ qq) + ((2 * blk + (p >> 1)) ^ (2 * e + h))) + 8 * (p & 1) + 16384;
    }
    const float csc = 0.08838834764831845f * 1.4426950408889634f;
    float m_run = -__builtin_inff(), l_run = 0.f;
    f32x16 o[4];
#pragma unroll
    for (int dt = 0; dt < 4; ++dt)
#pragma unroll
      for (int i = 0; i < 16; ++i) o[dt][i] = 0.f;

    for (int t = 0; t < 8; ++t) {
      barrier_lgkm();
      const char* slot = smem + LDS_RING + (t & 3) * 32768;
      f32x16 s0, s1;
#pragma unroll
      for (int i = 0; i < 16; ++i) { s0[i] = 0.f; s1[i] = 0.f; }
#pragma unroll
      for (int s = 0; s < 8; ++s) {
        const bf16x8 k0 = *(const bf16x8*)(slot + kaddr[s]);
        const bf16x8 k1 = *(const bf16x8*)(slot + kaddr[s] + 8192);
        s0 = __builtin_amdgcn_mfma_f32_32x32x16_bf16(k0, qf[s], s0, 0, 0, 0);
        s1 = __builtin_amdgcn_mfma_f32_32x32x16_bf16(k1, qf[s], s1, 0, 0, 0);
      }
      float mx = s0[0];
#pragma unroll
      for (int i = 1; i < 16; ++i) mx = fmaxf(mx, s0[i]);
#pragma unroll
      for (int i = 0; i < 16; ++i) mx = fmaxf(mx, s1[i]);
      {
        auto rr = __builtin_amdgcn_permlane32_swap(__float_as_uint(mx), __float_as_uint(mx), false, false);
        mx = fmaxf(__uint_as_float(rr[0]), __uint_as_float(rr[1]));
      }
      const float m_new = fmaxf(m_run, mx);
      const float alpha = __builtin_amdgcn_exp2f((m_run - m_new) * csc);
      const float nm = -m_new * csc;
      m_run = m_new;
      float ps = 0.f;
#pragma unroll
      for (int i = 0; i < 16; ++i) { s0[i] = __builtin_amdgcn_exp2f(fmaf(s0[i], csc, nm)); ps += s0[i]; }
#pragma unroll
      for (int i = 0; i < 16; ++i) { s1[i] = __builtin_amdgcn_exp2f(fmaf(s1[i], csc, nm)); ps += s1[i]; }
      l_run = l_run * alpha + ps;
      bf16x8 pf[4];
#pragma unroll
      for (int s2 = 0; s2 < 2; ++s2) {
        u32x4 a, c;
#pragma unroll
        for (int m = 0; m < 4; ++m) {
          a[m] = cvtpk(s0[8 * s2 + 2 * m], s0[8 * s2 + 2 * m + 1]);
          c[m] = cvtpk(s1[8 * s2 + 2 * m], s1[8 * s2 + 2 * m + 1]);
        }
        pf[s2] = __builtin_bit_cast(bf16x8, a);
        pf[2 + s2] = __builtin_bit_cast(bf16x8, c);
      }
#pragma unroll
      for (int dt = 0; dt < 4; ++dt)
#pragma unroll
        for (int i = 0; i < 16; ++i) o[dt][i] *= alpha;
#pragma unroll
      for (int dt = 0; dt < 4; ++dt) {
#pragma unroll
        for (int kk = 0; kk < 4; ++kk) {
          const s16x4 lo = __builtin_amdgcn_ds_read_tr16_b64_v4i16((LDS3 s16x4*)(slot + vaddr[dt * 2 + 0] + 4096 * kk));
          const s16x4 hi = __builtin_amdgcn_ds_read_tr16_b64_v4i16((LDS3 s16x4*)(slot + vaddr[dt * 2 + 1] + 4096 * kk));
          const bf16x8 vf = __builtin_shufflevector(lo, hi, 0, 1, 2, 3, 4, 5, 6, 7);
          o[dt] = __builtin_amdgcn_mfma_f32_32x32x16_bf16(vf, pf[kk], o[dt], 0, 0, 0);
        }
      }
    }
    STAMP(3);
    {
      auto rr = __builtin_amdgcn_permlane32_swap(__float_as_uint(l_run), __float_as_uint(l_run), false, false);
      l_run = __uint_as_float(rr[0]) + __uint_as_float(rr[1]);
    }
    const float inv = 1.0f / l_run;
#pragma unroll
    for (int dt = 0; dt < 4; ++dt)
#pragma unroll
      for (int a = 0; a < 4; ++a) {
        u32x2 v;
        v[0] = cvtpk(o[dt][4 * a] * inv, o[dt][4 * a + 1] * inv);
        v[1] = cvtpk(o[dt][4 * a + 2] * inv, o[dt][4 * a + 3] * inv);
        const int c = 16 * w4 + 4 * dt + a;
        *(u32x2*)(smem + LDS_X + r32 * 1024 + 16 * (c ^ (r32 & 15)) + 8 * h) = v;
      }
    barrier_lgkm();
    STAMP(4);
    STAMP_FLUSH;
  } else {
    const unsigned sbase = (unsigned)(size_t)smem;
    {
      const u16* xp = x + (size_t)b * SQ * DM;
#pragma unroll
      for (int i = 0; i < 8; ++i) {
        const int row = 8 * w4 + i;
        const int c = (lane & ~15) | ((lane & 15) ^ (row & 15));
        glds16(xp, (unsigned)(row * DM + c * 8) * 2, sbase + LDS_X + row * 1024);
      }
    }
    const u16* kp = kc + (size_t)(b * HKV + hkv) * SK * DH;
    const u16* vp = vc + (size_t)(b * HKV + hkv) * SK * DH;
    unsigned koff[4], voff[4];
#pragma unroll
    for (int j = 0; j < 4; ++j) {
      const int row = 16 * w4 + 4 * j + g;
      koff[j] = (row * DH + 8 * (r16 ^ (row & 15))) * 2;
      voff[j] = (row * DH + 8 * (r16 ^ (((row & 3) << 2) | ((row >> 2) & 3)))) * 2;
    }
#define KV_BURST(t) do { \
    _Pragma("unroll") for (int j = 0; j < 4; ++j) glds16_nt(kp + (t) * 64 * DH, koff[j], sbase + LDS_RING + ((t) & 3) * 32768 + (16 * w4 + 4 * j) * 256); \
    _Pragma("unroll") for (int j = 0; j < 4; ++j) glds16_nt(vp + (t) * 64 * DH, voff[j], sbase + LDS_RING + ((t) & 3) * 32768 + 16384 + (16 * w4 + 4 * j) * 256); \
  } while (0)
    wait_vm<0>();
    barrier_lgkm();
    STAMP(1);
    KV_BURST(0);
    asm volatile("s_barrier" ::: "memory");
    KV_BURST(1);
    asm volatile("s_barrier" ::: "memory");
    KV_BURST(2);
    asm volatile("s_barrier" ::: "memory");
    KV_BURST(3);
    const u16* wp = wo + (size_t)(hkv * 512) * 512 + w4 * 128;
    const unsigned wv = (unsigned)(8 * g * 512 + r16 * 8) * 2;
    u32x4 raw[4][8];
    wait_vm<24>();
    barrier_lgkm();
    wload<512>(raw[0], wp, wv, 0);
    wait_vm<24>();
    barrier_lgkm();
    KV_BURST(4);
    wload<512>(raw[1], wp, wv, 1);
    wait_vm<32>();
    barrier_lgkm();
    KV_BURST(5);
    wload<512>(raw[2], wp, wv, 2);
    wait_vm<40>();
    barrier_lgkm();
    KV_BURST(6);
    wload<512>(raw[3], wp, wv, 3);
    wait_vm<40>();
    barrier_lgkm();
    KV_BURST(7);
    wait_vm<32>();
    barrier_lgkm();
    wait_vm<16>();
    barrier_lgkm();
    wait_vm<0>();
    barrier_lgkm();
    f32x4 acc[8][2];
#pragma unroll
    for (int t = 0; t < 8; ++t) { acc[t][0] = f32x4{0, 0, 0, 0}; acc[t][1] = f32x4{0, 0, 0, 0}; }
    STAMP(3);
    barrier_lgkm();
    STAMP(4);
#pragma unroll
    for (int s = 0; s < 16; ++s) {
      if (s < 13) wait_raw<24>(raw[s & 3]); else if (s == 13) wait_raw<16>(raw[s & 3]); else if (s == 14) wait_raw<8>(raw[s & 3]); else wait_raw<0>(raw[s & 3]);
      wstep(raw[s & 3], acc, ximg, s, boff);
      __builtin_amdgcn_sched_barrier(0);
      if (s + 4 < 16) wload<512>(raw[s & 3], wp, wv, s + 4);
    }
    STAMP(5);
    f32x4* pp = (f32x4*)part + (size_t)blockIdx.x * 4096 + w4 * 1024 + lane;
#pragma unroll
    for (int qt = 0; qt < 2; ++qt)
#pragma unroll
      for (int i = 0; i < 4; ++i) {
        f32x4 v0 = {acc[0][qt][i], acc[1][qt][i], acc[2][qt][i], acc[3][qt][i]};
        f32x4 v1 = {acc[4][qt][i], acc[5][qt][i], acc[6][qt][i], acc[7][qt][i]};
        pp[(qt * 4 + i) * 128] = v0;
        pp[(qt * 4 + i) * 128 + 64] = v1;
      }
    asm volatile("s_waitcnt vmcnt(0)" ::: "memory");
    STAMP(6);
    STAMP_FLUSH;
  }
}

__global__ __launch_bounds__(256) void reduce_kernel(const float* part, u16* out) {
  const int idx = blockIdx.x * 256 + threadIdx.x;
  const int b = idx >> 11, w4 = (idx >> 9) & 3, qi = (idx >> 6) & 7, lane = idx & 63;
  const f32x4* p = (const f32x4*)part + (size_t)b * 8 * 4096 + w4 * 1024 + qi * 128 + lane;
  f32x4 s0 = p[0], s1 = p[64];
#pragma unroll
  for (int h = 1; h < 8; ++h) { s0 += p[(size_t)h * 4096]; s1 += p[(size_t)h * 4096 + 64]; }
  u32x4 v;
  v[0] = cvtpk(s0[0], s0[1]);
  v[1] = cvtpk(s0[2], s0[3]);
  v[2] = cvtpk(s1[0], s1[1]);
  v[3] = cvtpk(s1[2], s1[3]);
  const int q = (lane & 15) + 16 * (qi >> 2), n = 128 * w4 + 32 * (lane >> 4) + 8 * (qi & 3);
  *(u32x4*)(out + (size_t)b * (SQ * DM) + q * DM + n) = v;
}

extern "C" void kernel_launch(void* const* d_in, const int* in_sizes, int n_in, void* d_out, int out_size, void* d_ws,
                              size_t ws_size, hipStream_t stream) {
  const u16* x = (const u16*)d_in[0];
  const u16* kc = (const u16*)d_in[1];
  const u16* vc = (const u16*)d_in[2];
  const u16* wq = (const u16*)d_in[3];
  const u16* wo = (const u16*)d_in[4];
  float* part = (float*)d_ws;
  (void)hipFuncSetAttribute((const void*)fused_kernel, hipFuncAttributeMaxDynamicSharedMemorySize, LDS_TOTAL);
  fused_kernel<<<256, 512, LDS_TOTAL, stream>>>(x, kc, vc, wq, wo, part);
  reduce_kernel<<<256, 256, 0, stream>>>(part, (u16*)d_out);
}
```

```cpp
#include <hip/hip_runtime.h>
#include <hip/hip_bf16.h>

typedef __attribute__((ext_vector_type(8))) short bf16x8;
typedef __attribute__((ext_vector_type(4))) short s16x4;
typedef __attribute__((ext_vector_type(4))) float f32x4;
typedef __attribute__((ext_vector_type(16))) float f32x16;
typedef __attribute__((ext_vector_type(4))) unsigned u32x4;
typedef __attribute__((ext_vector_type(2))) unsigned u32x2;
typedef unsigned short u16;

#define DI __device__ __forceinline__
#define LDS3 __attribute__((address_space(3)))

constexpr int B_ = 32, SQ = 32, DM = 512, HQ = 32, HKV = 8, SK = 512, DH = 128;
constexpr int LDS_RING = 0;
constexpr int LDS_X = 131072;
constexpr int LDS_TOTAL = 163840;

#define STAMP(i) do {} while (0)
#define STAMP_DECL
#define STAMP_FLUSH do {} while (0)

typedef __attribute__((ext_vector_type(2))) __bf16 bf16x2_t;
typedef __attribute__((ext_vector_type(2))) float f32x2;
DI unsigned cvtpk(float lo, float hi) {
  f32x2 v = {lo, hi};
  return __builtin_bit_cast(unsigned, __builtin_convertvector(v, bf16x2_t));
}

DI void barrier_lgkm() { asm volatile("s_waitcnt lgkmcnt(0)\n\ts_barrier" ::: "memory"); }
DI void barrier_raw() { asm volatile("s_barrier" ::: "memory"); }

DI void glds16(const void* sbase, unsigned voff, unsigned lds_dst) {
  unsigned keep;
  asm volatile("s_mov_b32 %0, m0\n\ts_mov_b32 m0, %3\n\ts_nop 0\n\tglobal_load_lds_dwordx4 %1, %2\n\ts_mov_b32 m0, %0"
               : "=&s"(keep) : "v"(voff), "s"(sbase), "s"(lds_dst) : "memory");
}
DI void glds16_nt(const void* sbase, unsigned voff, unsigned lds_dst) {
  unsigned keep;
  asm volatile("s_mov_b32 %0, m0\n\ts_mov_b32 m0, %3\n\ts_nop 0\n\tglobal_load_lds_dwordx4 %1, %2 nt\n\ts_mov_b32 m0, %0"
               : "=&s"(keep) : "v"(voff), "s"(sbase), "s"(lds_dst) : "memory");
}
DI void gload16(u32x4& dst, const void* sbase, unsigned voff) {
  asm volatile("global_load_dwordx4 %0, %1, %2" : "=v"(dst) : "v"(voff), "s"(sbase) : "memory");
}
template <int N>
DI void wait_raw(u32x4 (&r)[8]) {
  asm volatile("s_waitcnt vmcnt(%8)"
               : "+v"(r[0]), "+v"(r[1]), "+v"(r[2]), "+v"(r[3]), "+v"(r[4]), "+v"(r[5]), "+v"(r[6]), "+v"(r[7]) : "i"(N) : "memory");
}
template <int N> DI void wait_vm() { asm volatile("s_waitcnt vmcnt(%0)" :: "i"(N) : "memory"); }

template <int RS>
DI void wload(u32x4 (&raw)[8], const u16* p, unsigned voff, int ks) {
  const u16* q = p + (size_t)(32 * ks) * RS;
#pragma unroll
  for (int j = 0; j < 8; ++j) gload16(raw[j], q + (size_t)j * RS, voff);
}
DI void wstep(const u32x4 (&raw)[8], f32x4 (&acc)[8][2], const char* bimg, int ks, int r16, int g) {
  const unsigned boff = r16 * 1024 + (((4 * ks + g) ^ r16) << 4);
  const bf16x8 b0 = *(const bf16x8*)(bimg + boff);
  const bf16x8 b1 = *(const bf16x8*)(bimg + boff + 16384);
#pragma unroll
  for (int c = 0; c < 4; ++c) {
    u32x4 f0, f1;
#pragma unroll
    for (int m = 0; m < 4; ++m) {
      f0[m] = __builtin_amdgcn_perm(raw[2 * m + 1][c], raw[2 * m][c], 0x05040100u);
      f1[m] = __builtin_amdgcn_perm(raw[2 * m + 1][c], raw[2 * m][c], 0x07060302u);
    }
    const bf16x8 a0 = __builtin_bit_cast(bf16x8, f0), a1 = __builtin_bit_cast(bf16x8, f1);
    acc[2 * c][0] = __builtin_amdgcn_mfma_f32_16x16x32_bf16(a0, b0, acc[2 * c][0], 0, 0, 0);
    acc[2 * c][1] = __builtin_amdgcn_mfma_f32_16x16x32_bf16(a0, b1, acc[2 * c][1], 0, 0, 0);
    acc[2 * c + 1][0] = __builtin_amdgcn_mfma_f32_16x16x32_bf16(a1, b0, acc[2 * c + 1][0], 0, 0, 0);
    acc[2 * c + 1][1] = __builtin_amdgcn_mfma_f32_16x16x32_bf16(a1, b1, acc[2 * c + 1][1], 0, 0, 0);
  }
}
template <int RS, bool TRIG>
DI void proj_loop(u32x4 (&raw)[4][8], f32x4 (&acc)[8][2], const u16* wp, unsigned wv, const char* bimg, int rot, int r16, int g, int stag) {
#pragma unroll 1
  for (int it = 0; it < 3; ++it) {
#pragma unroll
    for (int u = 0; u < 4; ++u) {
      const int s = 4 * it + u;
      wait_raw<24>(raw[u]);
      wstep(raw[u], acc, bimg, (s + rot) & 15, r16, g);
      __builtin_amdgcn_sched_barrier(0);
      wload<RS>(raw[u], wp, wv, (s + 4 + rot) & 15);
      if (TRIG) { if (u == stag) barrier_raw(); }
    }
  }
  wait_raw<24>(raw[0]); wstep(raw[0], acc, bimg, (12 + rot) & 15, r16, g);
  wait_raw<16>(raw[1]); wstep(raw[1], acc, bimg, (13 + rot) & 15, r16, g);
  wait_raw<8>(raw[2]);  wstep(raw[2], acc, bimg, (14 + rot) & 15, r16, g);
  wait_raw<0>(raw[3]);  wstep(raw[3], acc, bimg, (15 + rot) & 15, r16, g);
}

__global__ __launch_bounds__(512, 2) void fused_kernel(const u16* x, const u16* kc, const u16* vc, const u16* wq, const u16* wo,
                                                       float* part
) {
  extern __shared__ __attribute__((aligned(16))) char smem[];
  const int tid = threadIdx.x, lane = tid & 63;
  const int wave = __builtin_amdgcn_readfirstlane(tid >> 6);
  const int hkv = blockIdx.x & 7, b = blockIdx.x >> 3;
  const int w4 = wave & 3;
  const bool isW = wave >= 4;
  const int r16 = lane & 15, g = lane >> 4;
  const int r32 = lane & 31, h = lane >> 5;
  STAMP_DECL
  STAMP(0);
  const char* ximg = smem + LDS_X;
  const int stag = b & 3;
  const int rot = b & 15;
  const unsigned sbase = (unsigned)(size_t)smem;
  const u16* kp = kc + (size_t)(b * HKV + hkv) * SK * DH;
  const u16* vp = vc + (size_t)(b * HKV + hkv) * SK * DH;
#define KV_BURST(t) do { \
    int ln_ = lane; asm volatile("" : "+v"(ln_)); \
    const int r_ = ln_ & 15, g_ = ln_ >> 4; \
    _Pragma("unroll") for (int j = 0; j < 4; ++j) { const int row = 16 * w4 + 4 * j + g_; \
      glds16_nt(kp + (t) * 64 * DH, (row * DH + 8 * (r_ ^ (row & 15))) * 2, sbase + LDS_RING + ((t) & 3) * 32768 + (16 * w4 + 4 * j) * 256); } \
    _Pragma("unroll") for (int j = 0; j < 4; ++j) { const int row = 16 * w4 + 4 * j + g_; \
      glds16_nt(vp + (t) * 64 * DH, (row * DH + 8 * (r_ ^ (((row & 3) << 2) | ((row >> 2) & 3)))) * 2, sbase + LDS_RING + ((t) & 3) * 32768 + 16384 + (16 * w4 + 4 * j) * 256); } \
  } while (0)

  bf16x8 qf[8];
  char* qx = smem + LDS_X + w4 * 8192 + lane * 16;
  if (!isW) {
    const u16* wp = wq + hkv * 512 + w4 * 128;
    const unsigned wv = (unsigned)(8 * g * 4096 + r16 * 8) * 2;
    u32x4 raw[4][8];
#pragma unroll
    for (int u = 0; u < 4; ++u) wload<4096>(raw[u], wp, wv, (u + rot) & 15);
    f32x4 acc[8][2];
#pragma unroll
    for (int t = 0; t < 8; ++t) { acc[t][0] = f32x4{0, 0, 0, 0}; acc[t][1] = f32x4{0, 0, 0, 0}; }
    barrier_lgkm();
    STAMP(1);
    proj_loop<4096, true>(raw, acc, wp, wv, ximg, rot, r16, g, stag);
#pragma unroll
    for (int i = 0; i < 4; ++i) {
      u32x4 a, bb;
#pragma unroll
      for (int m = 0; m < 4; ++m) {
        unsigned a_ = cvtpk(acc[2 * m][0][i], acc[2 * m + 1][0][i]);
        unsigned b_ = cvtpk(acc[2 * m][1][i], acc[2 * m + 1][1][i]);
        auto r = __builtin_amdgcn_permlane16_swap(a_, b_, false, false);
        a[m] = r[0]; bb[m] = r[1];
      }
      qf[i] = __builtin_bit_cast(bf16x8, a);
      qf[4 + i] = __builtin_bit_cast(bf16x8, bb);
    }
    barrier_lgkm();
#pragma unroll
    for (int s = 0; s < 8; ++s) *(bf16x8*)(qx + s * 1024) = qf[s];
    STAMP(2);
    barrier_lgkm();
  } else {
    {
      const u16* xp = x + (size_t)b * SQ * DM;
#pragma unroll
      for (int i = 0; i < 8; ++i) {
        const int row = 8 * w4 + i;
        const int c = (lane & ~15) | ((lane & 15) ^ (row & 15));
        glds16(xp, (unsigned)(row * DM + c * 8) * 2, sbase + LDS_X + row * 1024);
      }
    }
    wait_vm<0>();
    barrier_lgkm();
    STAMP(1);
#pragma unroll 1
    for (int t = 0; t < 4; ++t) { KV_BURST(t); barrier_raw(); }
    wait_vm<16>();
    STAMP(2);
    barrier_lgkm();
#pragma unroll
    for (int s = 0; s < 8; ++s) qf[s] = *(const bf16x8*)(qx + s * 1024);
  }

  unsigned kaddr[8], vaddr[8];
#pragma unroll
  for (int s = 0; s < 8; ++s) kaddr[s] = r32 * 256 + 16 * ((8 * h + s) ^ (r32 & 15));
  {
    const int blk = (lane >> 4) & 1, qq = (lane & 15) >> 2, p = lane & 3;
#pragma unroll
    for (int dt = 0; dt < 4; ++dt)
#pragma unroll
      for (int e = 0; e < 2; ++e)
        vaddr[dt * 2 + e] = (8 * e + 4 * h + qq) * 256 + 16 * (4 * (dt ^ qq) + ((2 * blk + (p >> 1)) ^ (2 * e + h))) + 8 * (p & 1) + 16384;
  }
  const float csc = 0.08838834764831845f * 1.4426950408889634f;
  float m_run = -__builtin_inff(), l_run = 0.f;
  f32x16 o[4];
#pragma unroll
  for (int dt = 0; dt < 4; ++dt)
#pragma unroll
    for (int i = 0; i < 16; ++i) o[dt][i] = 0.f;

#pragma unroll 1
  for (int j = 0; j < 4; ++j) {
    const char* slot = smem + LDS_RING + ((2 * j + (isW ? 1 : 0)) & 3) * 32768;
    f32x16 s0, s1;
#pragma unroll
    for (int i = 0; i < 16; ++i) { s0[i] = 0.f; s1[i] = 0.f; }
#pragma unroll
    for (int s = 0; s < 8; ++s) {
      const bf16x8 k0 = *(const bf16x8*)(slot + kaddr[s]);
      const bf16x8 k1 = *(const bf16x8*)(slot + kaddr[s] + 8192);
      s0 = __builtin_amdgcn_mfma_f32_32x32x16_bf16(k0, qf[s], s0, 0, 0, 0);
      s1 = __builtin_amdgcn_mfma_f32_32x32x16_bf16(k1, qf[s], s1, 0, 0, 0);
    }
    float mx = s0[0];
#pragma unroll
    for (int i = 1; i < 16; ++i) mx = fmaxf(mx, s0[i]);
#pragma unroll
    for (int i = 0; i < 16; ++i) mx = fmaxf(mx, s1[i]);
    {
      auto rr = __builtin_amdgcn_permlane32_swap(__float_as_uint(mx), __float_as_uint(mx), false, false);
      mx = fmaxf(__uint_as_float(rr[0]), __uint_as_float(rr[1]));
    }
    const float m_new = fmaxf(m_run, mx);
    const float alpha = __builtin_amdgcn_exp2f((m_run - m_new) * csc);
    const float nm = -m_new * csc;
    m_run = m_new;
    float ps = 0.f;
#pragma unroll
    for (int i = 0; i < 16; ++i) { s0[i] = __builtin_amdgcn_exp2f(fmaf(s0[i], csc, nm)); ps += s0[i]; }
#pragma unroll
    for (int i = 0; i < 16; ++i) { s1[i] = __builtin_amdgcn_exp2f(fmaf(s1[i], csc, nm)); ps += s1[i]; }
    l_run = l_run * alpha + ps;
    bf16x8 pf[4];
#pragma unroll
    for (int s2 = 0; s2 < 2; ++s2) {
      u32x4 a, c;
#pragma unroll
      for (int m = 0; m < 4; ++m) {
        a[m] = cvtpk(s0[8 * s2 + 2 * m], s0[8 * s2 + 2 * m + 1]);
        c[m] = cvtpk(s1[8 * s2 + 2 * m], s1[8 * s2 + 2 * m + 1]);
      }
      pf[s2] = __builtin_bit_cast(bf16x8, a);
      pf[2 + s2] = __builtin_bit_cast(bf16x8, c);
    }
#pragma unroll
    for (int dt = 0; dt < 4; ++dt)
#pragma unroll
      for (int i = 0; i < 16; ++i) o[dt][i] *= alpha;
#pragma unroll
    for (int dt = 0; dt < 4; ++dt) {
#pragma unroll
      for (int kk = 0; kk < 4; ++kk) {
        const s16x4 lo = __builtin_amdgcn_ds_read_tr16_b64_v4i16((LDS3 s16x4*)(slot + vaddr[dt * 2 + 0] + 4096 * kk));
        const s16x4 hi = __builtin_amdgcn_ds_read_tr16_b64_v4i16((LDS3 s16x4*)(slot + vaddr[dt * 2 + 1] + 4096 * kk));
        const bf16x8 vf = __builtin_shufflevector(lo, hi, 0, 1, 2, 3, 4, 5, 6, 7);
        o[dt] = __builtin_amdgcn_mfma_f32_32x32x16_bf16(vf, pf[kk], o[dt], 0, 0, 0);
      }
    }
    if (j < 3) {
      if (isW) wait_vm<0>();
      barrier_lgkm();
      if (isW && j < 2) {
#pragma unroll 1
        for (int t = 2 * j + 4; t < 2 * j + 6; ++t) KV_BURST(t);
      }
    }
  }
  STAMP(3);
  {
    auto rr = __builtin_amdgcn_permlane32_swap(__float_as_uint(l_run), __float_as_uint(l_run), false, false);
    l_run = __uint_as_float(rr[0]) + __uint_as_float(rr[1]);
  }
  char* mg = smem + LDS_RING + w4 * 16384 + lane * 16;
  char* mlp = smem + LDS_X + h * 1024 + 256 * w4 + 8 * r32;
  if (isW) {
#pragma unroll
    for (int dt = 0; dt < 4; ++dt)
#pragma unroll
      for (int a = 0; a < 4; ++a)
        *(f32x4*)(mg + (dt * 4 + a) * 1024) = f32x4{o[dt][4 * a], o[dt][4 * a + 1], o[dt][4 * a + 2], o[dt][4 * a + 3]};
    *(f32x2*)mlp = f32x2{m_run, l_run};
    barrier_lgkm();
    const u16* wp = wo + (size_t)(hkv * 512) * 512 + w4 * 128;
    const unsigned wv = (unsigned)(8 * g * 512 + r16 * 8) * 2;
    u32x4 raw[4][8];
#pragma unroll
    for (int u = 0; u < 4; ++u) wload<512>(raw[u], wp, wv, (u + rot) & 15);
    f32x4 acc[8][2];
#pragma unroll
    for (int t = 0; t < 8; ++t) { acc[t][0] = f32x4{0, 0, 0, 0}; acc[t][1] = f32x4{0, 0, 0, 0}; }
    barrier_lgkm();
    STAMP(4);
    proj_loop<512, false>(raw, acc, wp, wv, ximg, rot, r16, g, 0);
    STAMP(5);
    f32x4* pp = (f32x4*)part + (size_t)blockIdx.x * 4096 + w4 * 1024 + lane;
#pragma unroll
    for (int qt = 0; qt < 2; ++qt)
#pragma unroll
      for (int i = 0; i < 4; ++i) {
        f32x4 v0 = {acc[0][qt][i], acc[1][qt][i], acc[2][qt][i], acc[3][qt][i]};
        f32x4 v1 = {acc[4][qt][i], acc[5][qt][i], acc[6][qt][i], acc[7][qt][i]};
        pp[(qt * 4 + i) * 128] = v0;
        pp[(qt * 4 + i) * 128 + 64] = v1;
      }
    STAMP(6);
  } else {
    barrier_lgkm();
    const f32x2 ml = *(const f32x2*)mlp;
    asm volatile("s_waitcnt lgkmcnt(0)" ::: "memory");
    const float m = fmaxf(m_run, ml[0]);
    const float fa = __builtin_amdgcn_exp2f((m_run - m) * csc), fw = __builtin_amdgcn_exp2f((ml[0] - m) * csc);
    const float inv = 1.0f / (l_run * fa + ml[1] * fw);
    const float ca = fa * inv, cw = fw * inv;
#pragma unroll
    for (int dt = 0; dt < 4; ++dt)
#pragma unroll
      for (int a = 0; a < 4; ++a) {
        const f32x4 ow = *(const f32x4*)(mg + (dt * 4 + a) * 1024);
        u32x2 v;
        v[0] = cvtpk(o[dt][4 * a] * ca + ow[0] * cw, o[dt][4 * a + 1] * ca + ow[1] * cw);
        v[1] = cvtpk(o[dt][4 * a + 2] * ca + ow[2] * cw, o[dt][4 * a + 3] * ca + ow[3] * cw);
        const int c = 16 * w4 + 4 * dt + a;
        *(u32x2*)(smem + LDS_X + r32 * 1024 + 16 * (c ^ (r32 & 15)) + 8 * h) = v;
      }
    barrier_lgkm();
    STAMP(4);
  }
  STAMP_FLUSH;
}

__global__ __launch_bounds__(256) void reduce_kernel(const float* part, u16* out) {
  const int idx = blockIdx.x * 256 + threadIdx.x;
  const int b = idx >> 11, w4 = (idx >> 9) & 3, qi = (idx >> 6) & 7, lane = idx & 63;
  const f32x4* p = (const f32x4*)part + (size_t)b * 8 * 4096 + w4 * 1024 + qi * 128 + lane;
  f32x4 s0 = p[0], s1 = p[64];
#pragma unroll
  for (int h = 1; h < 8; ++h) { s0 += p[(size_t)h * 4096]; s1 += p[(size_t)h * 4096 + 64]; }
  u32x4 v;
  v[0] = cvtpk(s0[0], s0[1]);
  v[1] = cvtpk(s0[2], s0[3]);
  v[2] = cvtpk(s1[0], s1[1]);
  v[3] = cvtpk(s1[2], s1[3]);
  const int q = (lane & 15) + 16 * (qi >> 2), n = 128 * w4 + 32 * (lane >> 4) + 8 * (qi & 3);
  *(u32x4*)(out + (size_t)b * (SQ * DM) + q * DM + n) = v;
}

extern "C" void kernel_launch(void* const* d_in, const int* in_sizes, int n_in, void* d_out, int out_size, void* d_ws,
                              size_t ws_size, hipStream_t stream) {
  const u16* x = (const u16*)d_in[0];
  const u16* kc = (const u16*)d_in[1];
  const u16* vc = (const u16*)d_in[2];
  const u16* wq = (const u16*)d_in[3];
  const u16* wo = (const u16*)d_in[4];
  float* part = (float*)d_ws;
  (void)hipFuncSetAttribute((const void*)fused_kernel, hipFuncAttributeMaxDynamicSharedMemorySize, LDS_TOTAL);
  fused_kernel<<<256, 512, LDS_TOTAL, stream>>>(x, kc, vc, wq, wo, part);
  reduce_kernel<<<256, 256, 0, stream>>>(part, (u16*)d_out);
}
```

```cpp
#include <hip/hip_runtime.h>
#include <hip/hip_bf16.h>

typedef __attribute__((ext_vector_type(8))) short bf16x8;
typedef __attribute__((ext_vector_type(4))) short s16x4;
typedef __attribute__((ext_vector_type(4))) float f32x4;
typedef __attribute__((ext_vector_type(16))) float f32x16;
typedef __attribute__((ext_vector_type(4))) unsigned u32x4;
typedef __attribute__((ext_vector_type(2))) unsigned u32x2;
typedef unsigned short u16;

#define DI __device__ __forceinline__
#define LDS3 __attribute__((address_space(3)))

constexpr int B_ = 32, SQ = 32, DM = 512, HQ = 32, HKV = 8, SK = 512, DH = 128;
constexpr int LDS_RING = 0;
constexpr int LDS_X = 131072;
constexpr int LDS_TOTAL = 163840;

#define STAMP(i) do {} while (0)
#define STAMP_DECL
#define STAMP_FLUSH do {} while (0)

typedef __attribute__((ext_vector_type(2))) __bf16 bf16x2_t;
typedef __attribute__((ext_vector_type(2))) float f32x2;
DI unsigned cvtpk(float lo, float hi) {
  f32x2 v = {lo, hi};
  return __builtin_bit_cast(unsigned, __builtin_convertvector(v, bf16x2_t));
}

DI void barrier_lgkm() { asm volatile("s_waitcnt lgkmcnt(0)\n\ts_barrier" ::: "memory"); }
DI void barrier_raw() { asm volatile("s_barrier" ::: "memory"); }

DI void glds16(const void* sbase, unsigned voff, unsigned lds_dst) {
  unsigned keep;
  asm volatile("s_mov_b32 %0, m0\n\ts_mov_b32 m0, %3\n\ts_nop 0\n\tglobal_load_lds_dwordx4 %1, %2\n\ts_mov_b32 m0, %0"
               : "=&s"(keep) : "v"(voff), "s"(sbase), "s"(lds_dst) : "memory");
}
DI void glds16_nt(const void* sbase, unsigned voff, unsigned lds_dst) {
  unsigned keep;
  asm volatile("s_mov_b32 %0, m0\n\ts_mov_b32 m0, %3\n\ts_nop 0\n\tglobal_load_lds_dwordx4 %1, %2 nt\n\ts_mov_b32 m0, %0"
               : "=&s"(keep) : "v"(voff), "s"(sbase), "s"(lds_dst) : "memory");
}
DI void gload16(u32x4& dst, const void* sbase, unsigned voff) {
  asm volatile("global_load_dwordx4 %0, %1, %2" : "=v"(dst) : "v"(voff), "s"(sbase) : "memory");
}
template <int N>
DI void wait_raw(u32x4 (&r)[8]) {
  asm volatile("s_waitcnt vmcnt(%8)"
               : "+v"(r[0]), "+v"(r[1]), "+v"(r[2]), "+v"(r[3]), "+v"(r[4]), "+v"(r[5]), "+v"(r[6]), "+v"(r[7]) : "i"(N) : "memory");
}
template <int N> DI void wait_vm() { asm volatile("s_waitcnt vmcnt(%0)" :: "i"(N) : "memory"); }

template <int RS>
DI void wload(u32x4 (&raw)[8], const u16* p, unsigned voff, int ks) {
  const u16* q = p + (size_t)(32 * ks) * RS;
#pragma unroll
  for (int j = 0; j < 8; ++j) gload16(raw[j], q + (size_t)j * RS, voff);
}
DI void wstep(const u32x4 (&raw)[8], f32x4 (&acc)[8][2], const char* bimg, int ks, int r16, int g) {
  const unsigned boff = r16 * 1024 + (((4 * ks + g) ^ r16) << 4);
  const bf16x8 b0 = *(const bf16x8*)(bimg + boff);
  const bf16x8 b1 = *(const bf16x8*)(bimg + boff + 16384);
#pragma unroll
  for (int c = 0; c < 4; ++c) {
    u32x4 f0, f1;
#pragma unroll
    for (int m = 0; m < 4; ++m) {
      f0[m] = __builtin_amdgcn_perm(raw[2 * m + 1][c], raw[2 * m][c], 0x05040100u);
      f1[m] = __builtin_amdgcn_perm(raw[2 * m + 1][c], raw[2 * m][c], 0x07060302u);
    }
    const bf16x8 a0 = __builtin_bit_cast(bf16x8, f0), a1 = __builtin_bit_cast(bf16x8, f1);
    acc[2 * c][0] = __builtin_amdgcn_mfma_f32_16x16x32_bf16(a0, b0, acc[2 * c][0], 0, 0, 0);
    acc[2 * c][1] = __builtin_amdgcn_mfma_f32_16x16x32_bf16(a0, b1, acc[2 * c][1], 0, 0, 0);
    acc[2 * c + 1][0] = __builtin_amdgcn_mfma_f32_16x16x32_bf16(a1, b0, acc[2 * c + 1][0], 0, 0, 0);
    acc[2 * c + 1][1] = __builtin_amdgcn_mfma_f32_16x16x32_bf16(a1, b1, acc[2 * c + 1][1], 0, 0, 0);
  }
}
template <int RS, bool TRIG>
DI void proj_loop(u32x4 (&raw)[4][8], f32x4 (&acc)[8][2], const u16* wp, unsigned wv, const char* bimg, int rot, int r16, int g, int stag) {
#pragma unroll 1
  for (int it = 0; it < 3; ++it) {
#pragma unroll
    for (int u = 0; u < 4; ++u) {
      const int s = 4 * it + u;
      wait_raw<24>(raw[u]);
      wstep(raw[u], acc, bimg, (s + rot) & 15, r16, g);
      __builtin_amdgcn_sched_barrier(0);
      wload<RS>(raw[u], wp, wv, (s + 4 + rot) & 15);
      if (TRIG) { if (u == stag) barrier_raw(); }
    }
  }
  wait_raw<24>(raw[0]); wstep(raw[0], acc, bimg, (12 + rot) & 15, r16, g);
  wait_raw<16>(raw[1]); wstep(raw[1], acc, bimg, (13 + rot) & 15, r16, g);
  wait_raw<8>(raw[2]);  wstep(raw[2], acc, bimg, (14 + rot) & 15, r16, g);
  wait_raw<0>(raw[3]);  wstep(raw[3], acc, bimg, (15 + rot) & 15, r16, g);
}

__global__ __launch_bounds__(512, 2) void fused_kernel(const u16* x, const u16* kc, const u16* vc, const u16* wq, const u16* wo,
                                                       float* part
) {
  extern __shared__ __attribute__((aligned(16))) char smem[];
  const int tid = threadIdx.x, lane = tid & 63;
  const int wave = __builtin_amdgcn_readfirstlane(tid >> 6);
  const int hkv = blockIdx.x & 7, b = blockIdx.x >> 3;
  const int w4 = wave & 3;
  const bool isW = wave >= 4;
  const int r16 = lane & 15, g = lane >> 4;
  const int r32 = lane & 31, h = lane >> 5;
  STAMP_DECL
  STAMP(0);
  const char* ximg = smem + LDS_X;
  const int stag = b & 3;
  const int rot = b & 15;
  const unsigned sbase = (unsigned)(size_t)smem;
  const u16* kp = kc + (size_t)(b * HKV + hkv) * SK * DH;
  const u16* vp = vc + (size_t)(b * HKV + hkv) * SK * DH;
#define KV_BURST(t) do { \
    int ln_ = lane; asm volatile("" : "+v"(ln_)); \
    const int r_ = ln_ & 15, g_ = ln_ >> 4; \
    _Pragma("unroll") for (int j = 0; j < 4; ++j) { const int row = 16 * w4 + 4 * j + g_; \
      glds16_nt(kp + (t) * 64 * DH, (row * DH + 8 * (r_ ^ (row & 15))) * 2, sbase + LDS_RING + ((t) & 3) * 32768 + (16 * w4 + 4 * j) * 256); } \
    _Pragma("unroll") for (int j = 0; j < 4; ++j) { const int row = 16 * w4 + 4 * j + g_; \
      glds16_nt(vp + (t) * 64 * DH, (row * DH + 8 * (r_ ^ (((row & 3) << 2) | ((row >> 2) & 3)))) * 2, sbase + LDS_RING + ((t) & 3) * 32768 + 16384 + (16 * w4 + 4 * j) * 256); } \
  } while (0)

  if (!isW) {
    {
      const u16* xp = x + (size_t)b * SQ * DM;
#pragma unroll
      for (int i = 0; i < 8; ++i) {
        const int row = 8 * w4 + i;
        const int c = (lane & ~15) | ((lane & 15) ^ (row & 15));
        glds16(xp, (unsigned)(row * DM + c * 8) * 2, sbase + LDS_X + row * 1024);
      }
    }
    bf16x8 qf[8];
    {
      const u16* wp = wq + hkv * 512 + w4 * 128;
      const unsigned wv = (unsigned)(8 * g * 4096 + r16 * 8) * 2;
      u32x4 raw[4][8];
#pragma unroll
      for (int u = 0; u < 4; ++u) wload<4096>(raw[u], wp, wv, (u + rot) & 15);
      f32x4 acc[8][2];
#pragma unroll
      for (int t = 0; t < 8; ++t) { acc[t][0] = f32x4{0, 0, 0, 0}; acc[t][1] = f32x4{0, 0, 0, 0}; }
      wait_vm<32>();
      barrier_lgkm();
      STAMP(1);
      proj_loop<4096, true>(raw, acc, wp, wv, ximg, rot, r16, g, stag);
#pragma unroll
      for (int i = 0; i < 4; ++i) {
        u32x4 a, bb;
#pragma unroll
        for (int m = 0; m < 4; ++m) {
          unsigned a_ = cvtpk(acc[2 * m][0][i], acc[2 * m + 1][0][i]);
          unsigned b_ = cvtpk(acc[2 * m][1][i], acc[2 * m + 1][1][i]);
          auto r = __builtin_amdgcn_permlane16_swap(a_, b_, false, false);
          a[m] = r[0]; bb[m] = r[1];
        }
        qf[i] = __builtin_bit_cast(bf16x8, a);
        qf[4 + i] = __builtin_bit_cast(bf16x8, bb);
      }
    }
    STAMP(2);
    unsigned kaddr[8], vaddr[8];
#pragma unroll
    for (int s = 0; s < 8; ++s) kaddr[s] = r32 * 256 + 16 * ((8 * h + s) ^ (r32 & 15));
    {
      const int blk = (lane >> 4) & 1, qq = (lane & 15) >> 2, p = lane & 3;
#pragma unroll
      for (int dt = 0; dt < 4; ++dt)
#pragma unroll
        for (int e = 0; e < 2; ++e)
          vaddr[dt * 2 + e] = (8 * e + 4 * h + qq) * 256 + 16 * (4 * (dt ^ qq) + ((2 * blk + (p >> 1)) ^ (2 * e + h))) + 8 * (p & 1) + 16384;
    }
    const float csc = 0.08838834764831845f * 1.4426950408889634f;
    float m_run = -__builtin_inff(), l_run = 0.f;
    f32x16 o[4];
#pragma unroll
    for (int dt = 0; dt < 4; ++dt)
#pragma unroll
      for (int i = 0; i < 16; ++i) o[dt][i] = 0.f;

#pragma unroll 1
    for (int j = 0; j < 4; ++j) {
      barrier_lgkm();
      const char* slot = smem + LDS_RING + (j & 1) * 65536;
      f32x16 sc[4];
#pragma unroll
      for (int u = 0; u < 4; ++u) {
#pragma unroll
        for (int i = 0; i < 16; ++i) sc[u][i] = 0.f;
#pragma unroll
        for (int s = 0; s < 8; ++s) {
          const bf16x8 kf = *(const bf16x8*)(slot + kaddr[s] + (u & 1) * 8192 + (u >> 1) * 32768);
          sc[u] = __builtin_amdgcn_mfma_f32_32x32x16_bf16(kf, qf[s], sc[u], 0, 0, 0);
        }
      }
      __builtin_amdgcn_sched_barrier(0);
      float mx = m_run;
#pragma unroll
      for (int u = 0; u < 4; ++u)
#pragma unroll
        for (int i = 0; i < 16; ++i) mx = fmaxf(mx, sc[u][i]);
      {
        auto rr = __builtin_amdgcn_permlane32_swap(__float_as_uint(mx), __float_as_uint(mx), false, false);
        mx = fmaxf(__uint_as_float(rr[0]), __uint_as_float(rr[1]));
      }
      const float alpha = __builtin_amdgcn_exp2f((m_run - mx) * csc);
      const float nm = -mx * csc;
      m_run = mx;
#pragma unroll
      for (int dt = 0; dt < 4; ++dt)
#pragma unroll
        for (int i = 0; i < 16; ++i) o[dt][i] *= alpha;
      float ps = 0.f;
      bf16x8 pf[2], vf[2][4];
#define PV_PREP(ks, buf) do { \
        const int u_ = (ks) >> 1, r0_ = 8 * ((ks) & 1); \
        const char* vb_ = slot + ((ks) >> 2) * 32768 + 4096 * ((ks) & 3); \
        _Pragma("unroll") for (int dt = 0; dt < 4; ++dt) { \
          const s16x4 lo = __builtin_amdgcn_ds_read_tr16_b64_v4i16((LDS3 s16x4*)(vb_ + vaddr[dt * 2 + 0])); \
          const s16x4 hi = __builtin_amdgcn_ds_read_tr16_b64_v4i16((LDS3 s16x4*)(vb_ + vaddr[dt * 2 + 1])); \
          vf[buf][dt] = __builtin_shufflevector(lo, hi, 0, 1, 2, 3, 4, 5, 6, 7); } \
        u32x4 pw_; \
        _Pragma("unroll") for (int m = 0; m < 4; ++m) { \
          const float p0 = __builtin_amdgcn_exp2f(fmaf(sc[u_][r0_ + 2 * m], csc, nm)); \
          const float p1 = __builtin_amdgcn_exp2f(fmaf(sc[u_][r0_ + 2 * m + 1], csc, nm)); \
          ps += p0 + p1; \
          pw_[m] = cvtpk(p0, p1); } \
        pf[buf] = __builtin_bit_cast(bf16x8, pw_); \
      } while (0)
      PV_PREP(0, 0);
#pragma unroll
      for (int ks = 0; ks < 8; ++ks) {
        __builtin_amdgcn_sched_barrier(0);
        if (ks < 7) PV_PREP(ks + 1, (ks + 1) & 1);
#pragma unroll
        for (int dt = 0; dt < 4; ++dt) o[dt] = __builtin_amdgcn_mfma_f32_32x32x16_bf16(vf[ks & 1][dt], pf[ks & 1], o[dt], 0, 0, 0);
      }
      l_run = l_run * alpha + ps;
    }
    STAMP(3);
    {
      auto rr = __builtin_amdgcn_permlane32_swap(__float_as_uint(l_run), __float_as_uint(l_run), false, false);
      l_run = __uint_as_float(rr[0]) + __uint_as_float(rr[1]);
    }
    const float inv = 1.0f / l_run;
#pragma unroll
    for (int dt = 0; dt < 4; ++dt)
#pragma unroll
      for (int a = 0; a < 4; ++a) {
        u32x2 v;
        v[0] = cvtpk(o[dt][4 * a] * inv, o[dt][4 * a + 1] * inv);
        v[1] = cvtpk(o[dt][4 * a + 2] * inv, o[dt][4 * a + 3] * inv);
        const int c = 16 * w4 + 4 * dt + a;
        *(u32x2*)(smem + LDS_X + r32 * 1024 + 16 * (c ^ (r32 & 15)) + 8 * h) = v;
      }
    barrier_lgkm();
    STAMP(4);
  } else {
    barrier_lgkm();
    STAMP(1);
#pragma unroll 1
    for (int t = 0; t < 3; ++t) { KV_BURST(t); barrier_raw(); }
    KV_BURST(3);
    const u16* wp = wo + (size_t)(hkv * 512) * 512 + w4 * 128;
    const unsigned wv = (unsigned)(8 * g * 512 + r16 * 8) * 2;
    u32x4 raw[4][8];
    wait_vm<16>();
    barrier_lgkm();
    wload<512>(raw[0], wp, wv, (0 + rot) & 15);
    wload<512>(raw[1], wp, wv, (1 + rot) & 15);
    wait_vm<16>();
    barrier_lgkm();
    KV_BURST(4);
    KV_BURST(5);
    wload<512>(raw[2], wp, wv, (2 + rot) & 15);
    wload<512>(raw[3], wp, wv, (3 + rot) & 15);
    wait_vm<16>();
    barrier_lgkm();
    KV_BURST(6);
    KV_BURST(7);
    wait_vm<0>();
    barrier_lgkm();
    f32x4 acc[8][2];
#pragma unroll
    for (int t = 0; t < 8; ++t) { acc[t][0] = f32x4{0, 0, 0, 0}; acc[t][1] = f32x4{0, 0, 0, 0}; }
    STAMP(3);
    barrier_lgkm();
    STAMP(4);
    proj_loop<512, false>(raw, acc, wp, wv, ximg, rot, r16, g, 0);
    STAMP(5);
    f32x4* pp = (f32x4*)part + (size_t)blockIdx.x * 4096 + w4 * 1024 + lane;
#pragma unroll
    for (int qt = 0; qt < 2; ++qt)
#pragma unroll
      for (int i = 0; i < 4; ++i) {
        f32x4 v0 = {acc[0][qt][i], acc[1][qt][i], acc[2][qt][i], acc[3][qt][i]};
        f32x4 v1 = {acc[4][qt][i], acc[5][qt][i], acc[6][qt][i], acc[7][qt][i]};
        pp[(qt * 4 + i) * 128] = v0;
        pp[(qt * 4 + i) * 128 + 64] = v1;
      }
    STAMP(6);
  }
  STAMP_FLUSH;
}

__global__ __launch_bounds__(256) void reduce_kernel(const float* part, u16* out) {
  const int idx = blockIdx.x * 256 + threadIdx.x;
  const int b = idx >> 11, w4 = (idx >> 9) & 3, qi = (idx >> 6) & 7, lane = idx & 63;
  const f32x4* p = (const f32x4*)part + (size_t)b * 8 * 4096 + w4 * 1024 + qi * 128 + lane;
  f32x4 s0 = p[0], s1 = p[64];
#pragma unroll
  for (int h = 1; h < 8; ++h) { s0 += p[(size_t)h * 4096]; s1 += p[(size_t)h * 4096 + 64]; }
  u32x4 v;
  v[0] = cvtpk(s0[0], s0[1]);
  v[1] = cvtpk(s0[2], s0[3]);
  v[2] = cvtpk(s1[0], s1[1]);
  v[3] = cvtpk(s1[2], s1[3]);
  const int q = (lane & 15) + 16 * (qi >> 2), n = 128 * w4 + 32 * (lane >> 4) + 8 * (qi & 3);
  *(u32x4*)(out + (size_t)b * (SQ * DM) + q * DM + n) = v;
}

extern "C" void kernel_launch(void* const* d_in, const int* in_sizes, int n_in, void* d_out, int out_size, void* d_ws,
                              size_t ws_size, hipStream_t stream) {
  const u16* x = (const u16*)d_in[0];
  const u16* kc = (const u16*)d_in[1];
  const u16* vc = (const u16*)d_in[2];
  const u16* wq = (const u16*)d_in[3];
  const u16* wo = (const u16*)d_in[4];
  float* part = (float*)d_ws;
  (void)hipFuncSetAttribute((const void*)fused_kernel, hipFuncAttributeMaxDynamicSharedMemorySize, LDS_TOTAL);
  fused_kernel<<<256, 512, LDS_TOTAL, stream>>>(x, kc, vc, wq, wo, part);
  reduce_kernel<<<256, 256, 0, stream>>>(part, (u16*)d_out);
}
```

```cpp
#include <hip/hip_runtime.h>
#include <hip/hip_bf16.h>

typedef __attribute__((ext_vector_type(8))) short bf16x8;
typedef __attribute__((ext_vector_type(4))) short s16x4;
typedef __attribute__((ext_vector_type(4))) float f32x4;
typedef __attribute__((ext_vector_type(16))) float f32x16;
typedef __attribute__((ext_vector_type(4))) unsigned u32x4;
typedef __attribute__((ext_vector_type(2))) unsigned u32x2;
typedef unsigned short u16;

#define DI __device__ __forceinline__
#define LDS3 __attribute__((address_space(3)))

constexpr int B_ = 32, SQ = 32, DM = 512, HQ = 32, HKV = 8, SK = 512, DH = 128;
constexpr int LDS_RING = 0;
constexpr int LDS_X = 131072;
constexpr int LDS_TOTAL = 163840;

#define STAMP(i) do {} while (0)
#define STAMP_DECL
#define STAMP_FLUSH do {} while (0)

typedef __attribute__((ext_vector_type(2))) __bf16 bf16x2_t;
typedef __attribute__((ext_vector_type(2))) float f32x2;
DI unsigned cvtpk(float lo, float hi) {
  f32x2 v = {lo, hi};
  return __builtin_bit_cast(unsigned, __builtin_convertvector(v, bf16x2_t));
}

DI void barrier_lgkm() { asm volatile("s_waitcnt lgkmcnt(0)\n\ts_barrier" ::: "memory"); }
DI void barrier_raw() { asm volatile("s_barrier" ::: "memory"); }

DI void glds16(const void* sbase, unsigned voff, unsigned lds_dst) {
  unsigned keep;
  asm volatile("s_mov_b32 %0, m0\n\ts_mov_b32 m0, %3\n\ts_nop 0\n\tglobal_load_lds_dwordx4 %1, %2\n\ts_mov_b32 m0, %0"
               : "=&s"(keep) : "v"(voff), "s"(sbase), "s"(lds_dst) : "memory");
}
DI void glds16_nt(const void* sbase, unsigned voff, unsigned lds_dst) {
  unsigned keep;
  asm volatile("s_mov_b32 %0, m0\n\ts_mov_b32 m0, %3\n\ts_nop 0\n\tglobal_load_lds_dwordx4 %1, %2 nt\n\ts_mov_b32 m0, %0"
               : "=&s"(keep) : "v"(voff), "s"(sbase), "s"(lds_dst) : "memory");
}
DI void gload16(u32x4& dst, const void* sbase, unsigned voff) {
  asm volatile("global_load_dwordx4 %0, %1, %2" : "=v"(dst) : "v"(voff), "s"(sbase) : "memory");
}
template <int N>
DI void wait_raw(u32x4 (&r)[8]) {
  asm volatile("s_waitcnt vmcnt(%8)"
               : "+v"(r[0]), "+v"(r[1]), "+v"(r[2]), "+v"(r[3]), "+v"(r[4]), "+v"(r[5]), "+v"(r[6]), "+v"(r[7]) : "i"(N) : "memory");
}
template <int N> DI void wait_vm() { asm volatile("s_waitcnt vmcnt(%0)" :: "i"(N) : "memory"); }

template <int RS>
DI void wload(u32x4 (&raw)[8], const u16* p, unsigned voff, int ks) {
  const u16* q = p + (size_t)(32 * ks) * RS;
#pragma unroll
  for (int j = 0; j < 8; ++j) gload16(raw[j], q + (size_t)j * RS, voff);
}
DI void wstep(const u32x4 (&raw)[8], f32x4 (&acc)[8][2], const char* bimg, int ks, int r16, int g) {
  const unsigned boff = r16 * 1024 + (((4 * ks + g) ^ r16) << 4);
  const bf16x8 b0 = *(const bf16x8*)(bimg + boff);
  const bf16x8 b1 = *(const bf16x8*)(bimg + boff + 16384);
#pragma unroll
  for (int c = 0; c < 4; ++c) {
    u32x4 f0, f1;
#pragma unroll
    for (int m = 0; m < 4; ++m) {
      f0[m] = __builtin_amdgcn_perm(raw[2 * m + 1][c], raw[2 * m][c], 0x05040100u);
      f1[m] = __builtin_amdgcn_perm(raw[2 * m + 1][c], raw[2 * m][c], 0x07060302u);
    }
    const bf16x8 a0 = __builtin_bit_cast(bf16x8, f0), a1 = __builtin_bit_cast(bf16x8, f1);
    acc[2 * c][0] = __builtin_amdgcn_mfma_f32_16x16x32_bf16(a0, b0, acc[2 * c][0], 0, 0, 0);
    acc[2 * c][1] = __builtin_amdgcn_mfma_f32_16x16x32_bf16(a0, b1, acc[2 * c][1], 0, 0, 0);
    acc[2 * c + 1][0] = __builtin_amdgcn_mfma_f32_16x16x32_bf16(a1, b0, acc[2 * c + 1][0], 0, 0, 0);
    acc[2 * c + 1][1] = __builtin_amdgcn_mfma_f32_16x16x32_bf16(a1, b1, acc[2 * c + 1][1], 0, 0, 0);
  }
}
template <int RS, bool TRIG>
DI void proj_loop(u32x4 (&raw)[4][8], f32x4 (&acc)[8][2], const u16* wp, unsigned wv, const char* bimg, int rot, int r16, int g, int stag) {
#pragma unroll 1
  for (int it = 0; it < 3; ++it) {
#pragma unroll
    for (int u = 0; u < 4; ++u) {
      const int s = 4 * it + u;
      wait_raw<24>(raw[u]);
      wstep(raw[u], acc, bimg, (s + rot) & 15, r16, g);
      __builtin_amdgcn_sched_barrier(0);
      wload<RS>(raw[u], wp, wv, (s + 4 + rot) & 15);
      if (TRIG) { if (u == stag) barrier_raw(); }
    }
  }
  wait_raw<24>(raw[0]); wstep(raw[0], acc, bimg, (12 + rot) & 15, r16, g);
  wait_raw<16>(raw[1]); wstep(raw[1], acc, bimg, (13 + rot) & 15, r16, g);
  wait_raw<8>(raw[2]);  wstep(raw[2], acc, bimg, (14 + rot) & 15, r16, g);
  wait_raw<0>(raw[3]);  wstep(raw[3], acc, bimg, (15 + rot) & 15, r16, g);
}

typedef __attribute__((address_space(1))) unsigned gu32;
#define RLX_AGENT __ATOMIC_RELAXED, __HIP_MEMORY_SCOPE_AGENT
constexpr size_t WS_XBUF = 0, XBUF_BYTES = (size_t)32 * 8 * 8 * 4096;
constexpr size_t WS_FLAG = XBUF_BYTES;
constexpr size_t WS_TOK = WS_FLAG + 256 * 128;

__global__ __launch_bounds__(512, 2) void fused_kernel(const u16* x, const u16* kc, const u16* vc, const u16* wq, const u16* wo,
                                                       char* ws, u16* out
) {
  extern __shared__ __attribute__((aligned(16))) char smem[];
  const int tid = threadIdx.x, lane = tid & 63;
  const int wave = __builtin_amdgcn_readfirstlane(tid >> 6);
  const int hkv = blockIdx.x & 7, b = blockIdx.x >> 3;
  const int w4 = wave & 3;
  const bool isW = wave >= 4;
  const int r16 = lane & 15, g = lane >> 4;
  const int r32 = lane & 31, h = lane >> 5;
  STAMP_DECL
  STAMP(0);
  const char* ximg = smem + LDS_X;
  const int stag = b & 3;
  const int rot = b & 15;
  unsigned tok = 0;
  const __amdgpu_buffer_rsrc_t xr = __builtin_amdgcn_make_buffer_rsrc(ws + WS_XBUF, 0, (int)XBUF_BYTES, 0x00020000);
  const unsigned sbase = (unsigned)(size_t)smem;
  const u16* kp = kc + (size_t)(b * HKV + hkv) * SK * DH;
  const u16* vp = vc + (size_t)(b * HKV + hkv) * SK * DH;
#define KV_BURST(t) do { \
    int ln_ = lane; asm volatile("" : "+v"(ln_)); \
    const int r_ = ln_ & 15, g_ = ln_ >> 4; \
    _Pragma("unroll") for (int j = 0; j < 4; ++j) { const int row = 16 * w4 + 4 * j + g_; \
      glds16_nt(kp + (t) * 64 * DH, (row * DH + 8 * (r_ ^ (row & 15))) * 2, sbase + LDS_RING + ((t) & 3) * 32768 + (16 * w4 + 4 * j) * 256); } \
    _Pragma("unroll") for (int j = 0; j < 4; ++j) { const int row = 16 * w4 + 4 * j + g_; \
      glds16_nt(vp + (t) * 64 * DH, (row * DH + 8 * (r_ ^ (((row & 3) << 2) | ((row >> 2) & 3)))) * 2, sbase + LDS_RING + ((t) & 3) * 32768 + 16384 + (16 * w4 + 4 * j) * 256); } \
  } while (0)

  if (!isW) {
    {
      const u16* xp = x + (size_t)b * SQ * DM;
#pragma unroll
      for (int i = 0; i < 8; ++i) {
        const int row = 8 * w4 + i;
        const int c = (lane & ~15) | ((lane & 15) ^ (row & 15));
        glds16(xp, (unsigned)(row * DM + c * 8) * 2, sbase + LDS_X + row * 1024);
      }
    }
    bf16x8 qf[8];
    {
      const u16* wp = wq + hkv * 512 + w4 * 128;
      const unsigned wv = (unsigned)(8 * g * 4096 + r16 * 8) * 2;
      u32x4 raw[4][8];
#pragma unroll
      for (int u = 0; u < 4; ++u) wload<4096>(raw[u], wp, wv, (u + rot) & 15);
      f32x4 acc[8][2];
#pragma unroll
      for (int t = 0; t < 8; ++t) { acc[t][0] = f32x4{0, 0, 0, 0}; acc[t][1] = f32x4{0, 0, 0, 0}; }
      wait_vm<32>();
      barrier_lgkm();
      STAMP(1);
      proj_loop<4096, true>(raw, acc, wp, wv, ximg, rot, r16, g, stag);
#pragma unroll
      for (int i = 0; i < 4; ++i) {
        u32x4 a, bb;
#pragma unroll
        for (int m = 0; m < 4; ++m) {
          unsigned a_ = cvtpk(acc[2 * m][0][i], acc[2 * m + 1][0][i]);
          unsigned b_ = cvtpk(acc[2 * m][1][i], acc[2 * m + 1][1][i]);
          auto r = __builtin_amdgcn_permlane16_swap(a_, b_, false, false);
          a[m] = r[0]; bb[m] = r[1];
        }
        qf[i] = __builtin_bit_cast(bf16x8, a);
        qf[4 + i] = __builtin_bit_cast(bf16x8, bb);
      }
    }
    STAMP(2);
    unsigned kaddr[8], vaddr[8];
#pragma unroll
    for (int s = 0; s < 8; ++s) kaddr[s] = r32 * 256 + 16 * ((8 * h + s) ^ (r32 & 15));
    {
      const int blk = (lane >> 4) & 1, qq = (lane & 15) >> 2, p = lane & 3;
#pragma unroll
      for (int dt = 0; dt < 4; ++dt)
#pragma unroll
        for (int e = 0; e < 2; ++e)
          vaddr[dt * 2 + e] = (8 * e + 4 * h + qq) * 256 + 16 * (4 * (dt ^ qq) + ((2 * blk + (p >> 1)) ^ (2 * e + h))) + 8 * (p & 1) + 16384;
    }
    const float csc = 0.08838834764831845f * 1.4426950408889634f;
    float m_run = -__builtin_inff(), l_run = 0.f;
    f32x16 o[4];
#pragma unroll
    for (int dt = 0; dt < 4; ++dt)
#pragma unroll
      for (int i = 0; i < 16; ++i) o[dt][i] = 0.f;

#pragma unroll 1
    for (int j = 0; j < 4; ++j) {
      barrier_lgkm();
      const char* slot = smem + LDS_RING + (j & 1) * 65536;
      f32x16 sc[4];
      bf16x8 kf[2][4];
      float mxa = m_run, mxb = m_run;
#define K_LOAD(gi, buf) do { _Pragma("unroll") for (int s_ = 0; s_ < 4; ++s_) \
        kf[buf][s_] = *(const bf16x8*)(slot + kaddr[4 * ((gi) & 1) + s_] + (((gi) >> 1) & 1) * 8192 + ((gi) >> 2) * 32768); } while (0)
      K_LOAD(0, 0);
#pragma unroll
      for (int gi = 0; gi < 8; ++gi) {
        const int u = gi >> 1;
        __builtin_amdgcn_sched_barrier(0);
        if (gi < 7) K_LOAD(gi + 1, (gi + 1) & 1);
        if ((gi & 1) == 0) {
#pragma unroll
          for (int i = 0; i < 16; ++i) sc[u][i] = 0.f;
        }
#pragma unroll
        for (int s = 0; s < 4; ++s) sc[u] = __builtin_amdgcn_mfma_f32_32x32x16_bf16(kf[gi & 1][s], qf[4 * (gi & 1) + s], sc[u], 0, 0, 0);
        if (gi >= 3 && (gi & 1)) {
#pragma unroll
          for (int i = 0; i < 8; ++i) { mxa = fmaxf(mxa, sc[u - 1][i]); mxb = fmaxf(mxb, sc[u - 1][8 + i]); }
        }
      }
      __builtin_amdgcn_sched_barrier(0);
#pragma unroll
      for (int i = 0; i < 8; ++i) { mxa = fmaxf(mxa, sc[3][i]); mxb = fmaxf(mxb, sc[3][8 + i]); }
      float mx = fmaxf(mxa, mxb);
      {
        auto rr = __builtin_amdgcn_permlane32_swap(__float_as_uint(mx), __float_as_uint(mx), false, false);
        mx = fmaxf(__uint_as_float(rr[0]), __uint_as_float(rr[1]));
      }
      const float alpha = __builtin_amdgcn_exp2f((m_run - mx) * csc);
      const float nm = -mx * csc;
      m_run = mx;
#pragma unroll
      for (int dt = 0; dt < 4; ++dt)
#pragma unroll
        for (int i = 0; i < 16; ++i) o[dt][i] *= alpha;
      float ps = 0.f;
      bf16x8 pf[2], vf[2][4];
#define PV_PREP(ks, buf) do { \
        const int u_ = (ks) >> 1, r0_ = 8 * ((ks) & 1); \
        const char* vb_ = slot + ((ks) >> 2) * 32768 + 4096 * ((ks) & 3); \
        _Pragma("unroll") for (int dt = 0; dt < 4; ++dt) { \
          const s16x4 lo = __builtin_amdgcn_ds_read_tr16_b64_v4i16((LDS3 s16x4*)(vb_ + vaddr[dt * 2 + 0])); \
          const s16x4 hi = __builtin_amdgcn_ds_read_tr16_b64_v4i16((LDS3 s16x4*)(vb_ + vaddr[dt * 2 + 1])); \
          vf[buf][dt] = __builtin_shufflevector(lo, hi, 0, 1, 2, 3, 4, 5, 6, 7); } \
        u32x4 pw_; \
        _Pragma("unroll") for (int m = 0; m < 4; ++m) { \
          const float p0 = __builtin_amdgcn_exp2f(fmaf(sc[u_][r0_ + 2 * m], csc, nm)); \
          const float p1 = __builtin_amdgcn_exp2f(fmaf(sc[u_][r0_ + 2 * m + 1], csc, nm)); \
          ps += p0 + p1; \
          pw_[m] = cvtpk(p0, p1); } \
        pf[buf] = __builtin_bit_cast(bf16x8, pw_); \
      } while (0)
      PV_PREP(0, 0);
#pragma unroll
      for (int ks = 0; ks < 8; ++ks) {
        __builtin_amdgcn_sched_barrier(0);
        if (ks < 7) PV_PREP(ks + 1, (ks + 1) & 1);
#pragma unroll
        for (int dt = 0; dt < 4; ++dt) o[dt] = __builtin_amdgcn_mfma_f32_32x32x16_bf16(vf[ks & 1][dt], pf[ks & 1], o[dt], 0, 0, 0);
      }
      l_run = l_run * alpha + ps;
    }
    STAMP(3);
    {
      auto rr = __builtin_amdgcn_permlane32_swap(__float_as_uint(l_run), __float_as_uint(l_run), false, false);
      l_run = __uint_as_float(rr[0]) + __uint_as_float(rr[1]);
    }
    const float inv = 1.0f / l_run;
#pragma unroll
    for (int dt = 0; dt < 4; ++dt)
#pragma unroll
      for (int a = 0; a < 4; ++a) {
        u32x2 v;
        v[0] = cvtpk(o[dt][4 * a] * inv, o[dt][4 * a + 1] * inv);
        v[1] = cvtpk(o[dt][4 * a + 2] * inv, o[dt][4 * a + 3] * inv);
        const int c = 16 * w4 + 4 * dt + a;
        *(u32x2*)(smem + LDS_X + r32 * 1024 + 16 * (c ^ (r32 & 15)) + 8 * h) = v;
      }
    barrier_lgkm();
    STAMP(4);
    if (wave == 0) {
      gu32* tokp = (gu32*)(ws + WS_TOK + (size_t)b * 128) + (lane & 7);
      gu32* inbox = (gu32*)(ws + WS_FLAG + (size_t)(b * 8 + hkv) * 128) + (lane & 7);
      gu32* outbox = (gu32*)(ws + WS_FLAG + (size_t)(b * 8 + (lane & 7)) * 128) + hkv;
      tok = __hip_atomic_load(tokp, RLX_AGENT);
      const unsigned mytok = __builtin_amdgcn_readlane(tok, hkv);
      barrier_lgkm();
      if (lane < 8) __hip_atomic_store(outbox, tok, RLX_AGENT);
      for (unsigned spin = 0; spin < (1u << 20); ++spin) {
        const unsigned f = __hip_atomic_load(inbox, RLX_AGENT);
        const unsigned t2 = __hip_atomic_load(tokp, RLX_AGENT);
        if (lane < 8 && t2 != tok) { tok = t2; __hip_atomic_store(outbox, tok, RLX_AGENT); }
        if (__all(f == mytok)) break;
        __builtin_amdgcn_s_sleep(1);
      }
      asm volatile("" ::: "memory");
    } else {
      barrier_lgkm();
    }
  } else {
    if (wave == 4) {
      gu32* inbox = (gu32*)(ws + WS_FLAG + (size_t)(b * 8 + hkv) * 128);
      gu32* mytokp = (gu32*)(ws + WS_TOK + (size_t)b * 128) + hkv;
      const unsigned v = __hip_atomic_load(lane < 8 ? inbox + lane : mytokp, RLX_AGENT);
      unsigned t = __builtin_amdgcn_readlane(v, 8) + 1;
      while (__any(v == t)) ++t;
      if (lane == 0) __hip_atomic_store(mytokp, t, RLX_AGENT);
      wait_vm<0>();
    }
    barrier_lgkm();
    STAMP(1);
#pragma unroll 1
    for (int t = 0; t < 3; ++t) { KV_BURST(t); barrier_raw(); }
    KV_BURST(3);
    const u16* wp = wo + (size_t)(hkv * 512) * 512 + w4 * 128;
    const unsigned wv = (unsigned)(8 * g * 512 + r16 * 8) * 2;
    u32x4 raw[4][8];
    wait_vm<16>();
    barrier_lgkm();
    wload<512>(raw[0], wp, wv, (0 + rot) & 15);
    wload<512>(raw[1], wp, wv, (1 + rot) & 15);
    wait_vm<16>();
    barrier_lgkm();
    KV_BURST(4);
    KV_BURST(5);
    wload<512>(raw[2], wp, wv, (2 + rot) & 15);
    wload<512>(raw[3], wp, wv, (3 + rot) & 15);
    wait_vm<16>();
    barrier_lgkm();
    KV_BURST(6);
    KV_BURST(7);
    wait_vm<0>();
    barrier_lgkm();
    f32x4 acc[8][2];
#pragma unroll
    for (int t = 0; t < 8; ++t) { acc[t][0] = f32x4{0, 0, 0, 0}; acc[t][1] = f32x4{0, 0, 0, 0}; }
    STAMP(3);
    barrier_lgkm();
    STAMP(4);
    proj_loop<512, false>(raw, acc, wp, wv, ximg, rot, r16, g, 0);
    STAMP(5);
    {
      const unsigned off = (unsigned)(((b * 8 + 2 * w4 + (g >> 1)) * 8 + hkv) * 4096 + (lane & 31) * 16);
#pragma unroll
      for (int qt = 0; qt < 2; ++qt)
#pragma unroll
        for (int i = 0; i < 4; ++i) {
          u32x4 v;
#pragma unroll
          for (int m = 0; m < 4; ++m) v[m] = cvtpk(acc[2 * m][qt][i], acc[2 * m + 1][qt][i]);
          __builtin_amdgcn_raw_buffer_store_b128(v, xr, off + (qt * 4 + i) * 512, 0, 16);
        }
    }
    wait_vm<0>();
    STAMP(6);
    barrier_lgkm();
  }
  barrier_lgkm();
  STAMP(7);
  {
    const unsigned off = (unsigned)(((b * 8 + hkv) * 8 + (lane >> 5)) * 4096 + wave * 512 + (lane & 31) * 16);
    u32x4 d[4];
#pragma unroll
    for (int p = 0; p < 4; ++p) d[p] = __builtin_amdgcn_raw_buffer_load_b128(xr, off + p * 8192, 0, 16);
    float sum[8];
#pragma unroll
    for (int m = 0; m < 4; ++m) {
      float lo = 0.f, hi = 0.f;
#pragma unroll
      for (int p = 0; p < 4; ++p) { lo += __uint_as_float(d[p][m] << 16); hi += __uint_as_float(d[p][m] & 0xffff0000u); }
      sum[2 * m] = lo; sum[2 * m + 1] = hi;
    }
#pragma unroll
    for (int e = 0; e < 8; ++e) {
      auto rr = __builtin_amdgcn_permlane32_swap(__float_as_uint(sum[e]), __float_as_uint(sum[e]), false, false);
      sum[e] = __uint_as_float(rr[0]) + __uint_as_float(rr[1]);
    }
    if (lane < 32) {
      u32x4 v;
#pragma unroll
      for (int m = 0; m < 4; ++m) v[m] = cvtpk(sum[2 * m], sum[2 * m + 1]);
      const int q = (lane & 15) + 16 * (wave >> 2), n = 64 * hkv + 32 * ((lane >> 4) & 1) + 8 * (wave & 3);
      *(u32x4*)(out + (size_t)b * (SQ * DM) + q * DM + n) = v;
    }
  }
  if (wave == 0) {
    gu32* tokp = (gu32*)(ws + WS_TOK + (size_t)b * 128) + (lane & 7);
    gu32* outbox = (gu32*)(ws + WS_FLAG + (size_t)(b * 8 + (lane & 7)) * 128) + hkv;
    const unsigned t2 = __hip_atomic_load(tokp, RLX_AGENT);
    if (lane < 8 && t2 != tok) __hip_atomic_store(outbox, t2, RLX_AGENT);
  }
  STAMP(8);
  STAMP_FLUSH;
}

extern "C" void kernel_launch(void* const* d_in, const int* in_sizes, int n_in, void* d_out, int out_size, void* d_ws,
                              size_t ws_size, hipStream_t stream) {
  const u16* x = (const u16*)d_in[0];
  const u16* kc = (const u16*)d_in[1];
  const u16* vc = (const u16*)d_in[2];
  const u16* wq = (const u16*)d_in[3];
  const u16* wo = (const u16*)d_in[4];
  (void)hipFuncSetAttribute((const void*)fused_kernel, hipFuncAttributeMaxDynamicSharedMemorySize, LDS_TOTAL);
  fused_kernel<<<256, 512, LDS_TOTAL, stream>>>(x, kc, vc, wq, wo, (char*)d_ws, (u16*)d_out);
}
```

```cpp
#include <hip/hip_runtime.h>
#include <hip/hip_bf16.h>

typedef __attribute__((ext_vector_type(8))) short bf16x8;
typedef __attribute__((ext_vector_type(4))) short s16x4;
typedef __attribute__((ext_vector_type(4))) float f32x4;
typedef __attribute__((ext_vector_type(16))) float f32x16;
typedef __attribute__((ext_vector_type(4))) unsigned u32x4;
typedef __attribute__((ext_vector_type(2))) unsigned u32x2;
typedef unsigned short u16;

#define DI __device__ __forceinline__
#define LDS3 __attribute__((address_space(3)))

constexpr int B_ = 32, SQ = 32, DM = 512, HQ = 32, HKV = 8, SK = 512, DH = 128;
constexpr int LDS_RING = 0;
constexpr int LDS_X = 131072;
constexpr int LDS_TOTAL = 163840;

#define STAMP(i) do {} while (0)
#define STAMP_DECL
#define STAMP_FLUSH do {} while (0)

typedef __attribute__((ext_vector_type(2))) __bf16 bf16x2_t;
typedef __attribute__((ext_vector_type(2))) float f32x2;
DI unsigned cvtpk(float lo, float hi) {
  f32x2 v = {lo, hi};
  return __builtin_bit_cast(unsigned, __builtin_convertvector(v, bf16x2_t));
}

DI void barrier_lgkm() { asm volatile("s_waitcnt lgkmcnt(0)\n\ts_barrier" ::: "memory"); }
DI void barrier_raw() { asm volatile("s_barrier" ::: "memory"); }

DI void glds16(const void* sbase, unsigned voff, unsigned lds_dst) {
  unsigned keep;
  asm volatile("s_mov_b32 %0, m0\n\ts_mov_b32 m0, %3\n\ts_nop 0\n\tglobal_load_lds_dwordx4 %1, %2\n\ts_mov_b32 m0, %0"
               : "=&s"(keep) : "v"(voff), "s"(sbase), "s"(lds_dst) : "memory");
}
DI void glds16_nt(const void* sbase, unsigned voff, unsigned lds_dst) {
  unsigned keep;
  asm volatile("s_mov_b32 %0, m0\n\ts_mov_b32 m0, %3\n\ts_nop 0\n\tglobal_load_lds_dwordx4 %1, %2 nt\n\ts_mov_b32 m0, %0"
               : "=&s"(keep) : "v"(voff), "s"(sbase), "s"(lds_dst) : "memory");
}
DI void gload16(u32x4& dst, const void* sbase, unsigned voff) {
  asm volatile("global_load_dwordx4 %0, %1, %2" : "=v"(dst) : "v"(voff), "s"(sbase) : "memory");
}
template <int N>
DI void wait_raw(u32x4 (&r)[8]) {
  asm volatile("s_waitcnt vmcnt(%8)"
               : "+v"(r[0]), "+v"(r[1]), "+v"(r[2]), "+v"(r[3]), "+v"(r[4]), "+v"(r[5]), "+v"(r[6]), "+v"(r[7]) : "i"(N) : "memory");
}
template <int N> DI void wait_vm() { asm volatile("s_waitcnt vmcnt(%0)" :: "i"(N) : "memory"); }

template <int RS>
DI void wload(u32x4 (&raw)[8], const u16* p, unsigned voff, int ks) {
  const u16* q = p + (size_t)(32 * ks) * RS;
#pragma unroll
  for (int j = 0; j < 8; ++j) gload16(raw[j], q + (size_t)j * RS, voff);
}
DI void wstep(const u32x4 (&raw)[8], f32x4 (&acc)[8][2], const char* bimg, int ks, int r16, int g) {
  const unsigned boff = r16 * 1024 + (((4 * ks + g) ^ r16) << 4);
  const bf16x8 b0 = *(const bf16x8*)(bimg + boff);
  const bf16x8 b1 = *(const bf16x8*)(bimg + boff + 16384);
#pragma unroll
  for (int c = 0; c < 4; ++c) {
    u32x4 f0, f1;
#pragma unroll
    for (int m = 0; m < 4; ++m) {
      f0[m] = __builtin_amdgcn_perm(raw[2 * m + 1][c], raw[2 * m][c], 0x05040100u);
      f1[m] = __builtin_amdgcn_perm(raw[2 * m + 1][c], raw[2 * m][c], 0x07060302u);
    }
    const bf16x8 a0 = __builtin_bit_cast(bf16x8, f0), a1 = __builtin_bit_cast(bf16x8, f1);
    acc[2 * c][0] = __builtin_amdgcn_mfma_f32_16x16x32_bf16(a0, b0, acc[2 * c][0], 0, 0, 0);
    acc[2 * c][1] = __builtin_amdgcn_mfma_f32_16x16x32_bf16(a0, b1, acc[2 * c][1], 0, 0, 0);
    acc[2 * c + 1][0] = __builtin_amdgcn_mfma_f32_16x16x32_bf16(a1, b0, acc[2 * c + 1][0], 0, 0, 0);
    acc[2 * c + 1][1] = __builtin_amdgcn_mfma_f32_16x16x32_bf16(a1, b1, acc[2 * c + 1][1], 0, 0, 0);
  }
}
DI int kstep(int i, int rot8, int kh) { return ((i + rot8) & 7) + 8 * kh; }

typedef __attribute__((address_space(1))) unsigned gu32;
#define RLX_AGENT __ATOMIC_RELAXED, __HIP_MEMORY_SCOPE_AGENT
constexpr size_t WS_XBUF = 0, XBUF_BYTES = (size_t)32 * 8 * 8 * 4096;
constexpr size_t WS_FLAG = XBUF_BYTES;
constexpr size_t WS_TOK = WS_FLAG + 256 * 128;

__global__ __launch_bounds__(512, 2) void fused_kernel(const u16* x, const u16* kc, const u16* vc, const u16* wq, const u16* wo,
                                                       char* ws, u16* out
) {
  extern __shared__ __attribute__((aligned(16))) char smem[];
  const int tid = threadIdx.x, lane = tid & 63;
  const int wave = __builtin_amdgcn_readfirstlane(tid >> 6);
  const int hkv = blockIdx.x & 7, b = blockIdx.x >> 3;
  const int w4 = wave & 3;
  const bool isW = wave >= 4;
  const int r16 = lane & 15, g = lane >> 4;
  const int r32 = lane & 31, h = lane >> 5;
  STAMP_DECL
  STAMP(0);
  const char* ximg = smem + LDS_X;
  const int stag = b & 3;
  const int rot = b & 15;
  unsigned tok = 0;
  const __amdgpu_buffer_rsrc_t xr = __builtin_amdgcn_make_buffer_rsrc(ws + WS_XBUF, 0, (int)XBUF_BYTES, 0x00020000);
  const unsigned sbase = (unsigned)(size_t)smem;
  const u16* kp = kc + (size_t)(b * HKV + hkv) * SK * DH;
  const u16* vp = vc + (size_t)(b * HKV + hkv) * SK * DH;
  const int rot8 = b & 7;
  const int kh = isW ? 1 : 0;
#define K_TILE(t) do { int ln_ = lane; asm volatile("" : "+v"(ln_)); const int r_ = ln_ & 15, g_ = ln_ >> 4; \
    _Pragma("unroll") for (int j = 0; j < 2; ++j) { const int row = 8 * wave + 4 * j + g_; \
      glds16_nt(kp + (t) * 64 * DH, (row * DH + 8 * (r_ ^ (row & 15))) * 2, sbase + LDS_RING + ((t) & 3) * 32768 + (8 * wave + 4 * j) * 256); } } while (0)
#define V_TILE(t) do { int ln_ = lane; asm volatile("" : "+v"(ln_)); const int r_ = ln_ & 15, g_ = ln_ >> 4; \
    _Pragma("unroll") for (int j = 0; j < 2; ++j) { const int row = 8 * wave + 4 * j + g_; \
      glds16_nt(vp + (t) * 64 * DH, (row * DH + 8 * (r_ ^ (((row & 3) << 2) | ((row >> 2) & 3)))) * 2, sbase + LDS_RING + ((t) & 3) * 32768 + 16384 + (8 * wave + 4 * j) * 256); } } while (0)
#define PROJ8(RS, wp, wv, bimg, HOOK) do { \
    _Pragma("unroll") for (int i = 0; i < 8; ++i) { \
      if (i < 6) wait_raw<16>(raw[i & 3]); else if (i == 6) wait_raw<8>(raw[i & 3]); else wait_raw<0>(raw[i & 3]); \
      if (i + 3 < 8) wload<RS>(raw[(i + 3) & 3], wp, wv, kstep(i + 3, rot8, kh)); \
      HOOK(i); \
      __builtin_amdgcn_sched_barrier(0); \
      wstep(raw[i & 3], acc, bimg, kstep(i, rot8, kh), r16, g); \
      __builtin_amdgcn_sched_barrier(0); \
    } } while (0)

  {
    const u16* xp = x + (size_t)b * SQ * DM;
#pragma unroll
    for (int i = 0; i < 4; ++i) {
      const int row = 4 * wave + i;
      const int c = (lane & ~15) | ((lane & 15) ^ (row & 15));
      glds16(xp, (unsigned)(row * DM + c * 8) * 2, sbase + LDS_X + row * 1024);
    }
  }
  bf16x8 qf[8];
  char* qx = smem + LDS_X + w4 * 8192 + lane * 16;
  char* px = smem + LDS_RING + w4 * 32768 + 16384 + lane * 16;
  {
    const u16* wp = wq + hkv * 512 + w4 * 128;
    const unsigned wv = (unsigned)(8 * g * 4096 + r16 * 8) * 2;
    u32x4 raw[4][8];
    wload<4096>(raw[0], wp, wv, kstep(0, rot8, kh));
    f32x4 acc[8][2];
#pragma unroll
    for (int t = 0; t < 8; ++t) { acc[t][0] = f32x4{0, 0, 0, 0}; acc[t][1] = f32x4{0, 0, 0, 0}; }
    wait_vm<8>();
    barrier_lgkm();
    STAMP(1);
    wload<4096>(raw[1], wp, wv, kstep(1, rot8, kh));
    wload<4096>(raw[2], wp, wv, kstep(2, rot8, kh));
#define KHOOK(i) do { if (((i) & 1) == (stag & 1)) K_TILE((i) >> 1); } while (0)
    PROJ8(4096, wp, wv, ximg, KHOOK);
    if (isW) {
#pragma unroll
      for (int t = 0; t < 8; ++t) { *(f32x4*)(px + (2 * t) * 1024) = acc[t][0]; *(f32x4*)(px + (2 * t + 1) * 1024) = acc[t][1]; }
    }
    wait_vm<0>();
    barrier_lgkm();
    if (!isW) {
#pragma unroll
      for (int t = 0; t < 8; ++t) { acc[t][0] += *(const f32x4*)(px + (2 * t) * 1024); acc[t][1] += *(const f32x4*)(px + (2 * t + 1) * 1024); }
#pragma unroll
      for (int i = 0; i < 4; ++i) {
        u32x4 a, bb;
#pragma unroll
        for (int m = 0; m < 4; ++m) {
          unsigned a_ = cvtpk(acc[2 * m][0][i], acc[2 * m + 1][0][i]);
          unsigned b_ = cvtpk(acc[2 * m][1][i], acc[2 * m + 1][1][i]);
          auto r = __builtin_amdgcn_permlane16_swap(a_, b_, false, false);
          a[m] = r[0]; bb[m] = r[1];
        }
        qf[i] = __builtin_bit_cast(bf16x8, a);
        qf[4 + i] = __builtin_bit_cast(bf16x8, bb);
      }
#pragma unroll
      for (int s = 0; s < 8; ++s) *(bf16x8*)(qx + s * 1024) = qf[s];
    }
    barrier_lgkm();
  }
  STAMP(2);
#pragma unroll 1
  for (int t = 0; t < 4; ++t) V_TILE(t);
  if (wave == 4) {
    gu32* inbox = (gu32*)(ws + WS_FLAG + (size_t)(b * 8 + hkv) * 128);
    gu32* mytokp = (gu32*)(ws + WS_TOK + (size_t)b * 128) + hkv;
    const unsigned v = __hip_atomic_load(lane < 8 ? inbox + lane : mytokp, RLX_AGENT);
    unsigned t = __builtin_amdgcn_readlane(v, 8) + 1;
    while (__any(v == t)) ++t;
    if (lane == 0) __hip_atomic_store(mytokp, t, RLX_AGENT);
  }
  if (isW) {
#pragma unroll
    for (int s = 0; s < 8; ++s) qf[s] = *(const bf16x8*)(qx + s * 1024);
  }

#define BOUNDARY(tau) do { wait_vm<0>(); barrier_lgkm(); \
    if ((tau) == 0) { K_TILE(4); K_TILE(5); } \
    else if ((tau) == 1) { V_TILE(4); V_TILE(5); K_TILE(6); K_TILE(7); } \
    else if ((tau) == 2) { V_TILE(6); V_TILE(7); } } while (0)
  unsigned kaddr[8], vaddr[8];
#pragma unroll
  for (int s = 0; s < 8; ++s) kaddr[s] = r32 * 256 + 16 * ((8 * h + s) ^ (r32 & 15)) + kh * 65536;
  {
    const int blk = (lane >> 4) & 1, qq = (lane & 15) >> 2, p = lane & 3;
#pragma unroll
    for (int dt = 0; dt < 4; ++dt)
#pragma unroll
      for (int e = 0; e < 2; ++e)
        vaddr[dt * 2 + e] = (8 * e + 4 * h + qq) * 256 + 16 * (4 * (dt ^ qq) + ((2 * blk + (p >> 1)) ^ (2 * e + h))) + 8 * (p & 1) + 16384 + kh * 65536;
  }
  const float csc = 0.08838834764831845f * 1.4426950408889634f;
  float m_run = -__builtin_inff(), l_run = 0.f;
  f32x16 o[4];
#pragma unroll
  for (int dt = 0; dt < 4; ++dt)
#pragma unroll
    for (int i = 0; i < 16; ++i) o[dt][i] = 0.f;
  const char* slot = smem + LDS_RING;
  if (isW) BOUNDARY(0);
#pragma unroll 1
  for (int ss = 0; ss < 2; ++ss) {
    f32x16 sc[4];
    bf16x8 kf[2][4];
    float mxa = m_run, mxb = m_run;
#define K_LOAD(gi, buf) do { _Pragma("unroll") for (int s_ = 0; s_ < 4; ++s_) \
      kf[buf][s_] = *(const bf16x8*)(slot + kaddr[4 * ((gi) & 1) + s_] + (((gi) >> 1) & 1) * 8192 + ((gi) >> 2) * 32768); } while (0)
    K_LOAD(0, 0);
#pragma unroll
    for (int gi = 0; gi < 8; ++gi) {
      const int u = gi >> 1;
      __builtin_amdgcn_sched_barrier(0);
      if (gi < 7) K_LOAD(gi + 1, (gi + 1) & 1);
      if ((gi & 1) == 0) {
#pragma unroll
        for (int i = 0; i < 16; ++i) sc[u][i] = 0.f;
      }
#pragma unroll
      for (int s = 0; s < 4; ++s) sc[u] = __builtin_amdgcn_mfma_f32_32x32x16_bf16(kf[gi & 1][s], qf[4 * (gi & 1) + s], sc[u], 0, 0, 0);
      if (gi >= 3 && (gi & 1)) {
#pragma unroll
        for (int i = 0; i < 8; ++i) { mxa = fmaxf(mxa, sc[u - 1][i]); mxb = fmaxf(mxb, sc[u - 1][8 + i]); }
      }
    }
    __builtin_amdgcn_sched_barrier(0);
#pragma unroll
    for (int i = 0; i < 8; ++i) { mxa = fmaxf(mxa, sc[3][i]); mxb = fmaxf(mxb, sc[3][8 + i]); }
    float mx = fmaxf(mxa, mxb);
    {
      auto rr = __builtin_amdgcn_permlane32_swap(__float_as_uint(mx), __float_as_uint(mx), false, false);
      mx = fmaxf(__uint_as_float(rr[0]), __uint_as_float(rr[1]));
    }
    const float alpha = __builtin_amdgcn_exp2f((m_run - mx) * csc);
    const float nm = -mx * csc;
    m_run = mx;
#pragma unroll
    for (int dt = 0; dt < 4; ++dt)
#pragma unroll
      for (int i = 0; i < 16; ++i) o[dt][i] *= alpha;
    BOUNDARY(2 * ss + kh);
    float ps = 0.f;
    bf16x8 pf[2], vf[2][4];
#define PV_PREP(ks, buf) do { \
      const int u_ = (ks) >> 1, r0_ = 8 * ((ks) & 1); \
      const char* vb_ = slot + ((ks) >> 2) * 32768 + 4096 * ((ks) & 3); \
      _Pragma("unroll") for (int dt = 0; dt < 4; ++dt) { \
        const s16x4 lo = __builtin_amdgcn_ds_read_tr16_b64_v4i16((LDS3 s16x4*)(vb_ + vaddr[dt * 2 + 0])); \
        const s16x4 hi = __builtin_amdgcn_ds_read_tr16_b64_v4i16((LDS3 s16x4*)(vb_ + vaddr[dt * 2 + 1])); \
        vf[buf][dt] = __builtin_shufflevector(lo, hi, 0, 1, 2, 3, 4, 5, 6, 7); } \
      u32x4 pw_; \
      _Pragma("unroll") for (int m = 0; m < 4; ++m) { \
        const float p0 = __builtin_amdgcn_exp2f(fmaf(sc[u_][r0_ + 2 * m], csc, nm)); \
        const float p1 = __builtin_amdgcn_exp2f(fmaf(sc[u_][r0_ + 2 * m + 1], csc, nm)); \
        ps += p0 + p1; \
        pw_[m] = cvtpk(p0, p1); } \
      pf[buf] = __builtin_bit_cast(bf16x8, pw_); \
    } while (0)
    PV_PREP(0, 0);
#pragma unroll
    for (int ks = 0; ks < 8; ++ks) {
      __builtin_amdgcn_sched_barrier(0);
      if (ks < 7) PV_PREP(ks + 1, (ks + 1) & 1);
#pragma unroll
      for (int dt = 0; dt < 4; ++dt) o[dt] = __builtin_amdgcn_mfma_f32_32x32x16_bf16(vf[ks & 1][dt], pf[ks & 1], o[dt], 0, 0, 0);
    }
    l_run = l_run * alpha + ps;
    if (!(isW && ss == 1)) BOUNDARY(2 * ss + 1 + kh);
  }
  STAMP(3);
  {
    auto rr = __builtin_amdgcn_permlane32_swap(__float_as_uint(l_run), __float_as_uint(l_run), false, false);
    l_run = __uint_as_float(rr[0]) + __uint_as_float(rr[1]);
  }
  char* mg = smem + LDS_RING + w4 * 16384 + lane * 16;
  char* mlp = smem + LDS_X + h * 1024 + 256 * w4 + 8 * r32;
  {
    const u16* wp = wo + (size_t)(hkv * 512) * 512 + w4 * 128;
    const unsigned wv = (unsigned)(8 * g * 512 + r16 * 8) * 2;
    u32x4 raw[4][8];
    if (!isW) {
#pragma unroll
      for (int dt = 0; dt < 4; ++dt)
#pragma unroll
        for (int a = 0; a < 4; ++a)
          *(f32x4*)(mg + (dt * 4 + a) * 1024) = f32x4{o[dt][4 * a], o[dt][4 * a + 1], o[dt][4 * a + 2], o[dt][4 * a + 3]};
      *(f32x2*)mlp = f32x2{m_run, l_run};
    } else {
      barrier_lgkm();
      const f32x2 ml = *(const f32x2*)mlp;
      asm volatile("s_waitcnt lgkmcnt(0)" ::: "memory");
      const float m = fmaxf(m_run, ml[0]);
      const float fw = __builtin_amdgcn_exp2f((m_run - m) * csc), fa = __builtin_amdgcn_exp2f((ml[0] - m) * csc);
      const float inv = 1.0f / (l_run * fw + ml[1] * fa);
      const float cw = fw * inv, ca = fa * inv;
#pragma unroll
      for (int dt = 0; dt < 4; ++dt)
#pragma unroll
        for (int a = 0; a < 4; ++a) {
          const f32x4 oa = *(const f32x4*)(mg + (dt * 4 + a) * 1024);
          u32x2 v;
          v[0] = cvtpk(o[dt][4 * a] * cw + oa[0] * ca, o[dt][4 * a + 1] * cw + oa[1] * ca);
          v[1] = cvtpk(o[dt][4 * a + 2] * cw + oa[2] * ca, o[dt][4 * a + 3] * cw + oa[3] * ca);
          const int c = 16 * w4 + 4 * dt + a;
          *(u32x2*)(smem + LDS_X + r32 * 1024 + 16 * (c ^ (r32 & 15)) + 8 * h) = v;
        }
    }
    __builtin_amdgcn_sched_barrier(0);
    wload<512>(raw[0], wp, wv, kstep(0, rot8, kh));
    wload<512>(raw[1], wp, wv, kstep(1, rot8, kh));
    wload<512>(raw[2], wp, wv, kstep(2, rot8, kh));
    if (!isW) barrier_lgkm();
    f32x4 acc[8][2];
#pragma unroll
    for (int t = 0; t < 8; ++t) { acc[t][0] = f32x4{0, 0, 0, 0}; acc[t][1] = f32x4{0, 0, 0, 0}; }
    barrier_lgkm();
    STAMP(4);
#define NOHOOK(i) do {} while (0)
    PROJ8(512, wp, wv, ximg, NOHOOK);
    STAMP(5);
    char* ox = smem + LDS_RING + 65536 + w4 * 16384 + lane * 16;
    if (!isW) {
#pragma unroll
      for (int t = 0; t < 8; ++t) { *(f32x4*)(ox + (2 * t) * 1024) = acc[t][0]; *(f32x4*)(ox + (2 * t + 1) * 1024) = acc[t][1]; }
      barrier_lgkm();
      if (wave == 0) {
        gu32* tokp = (gu32*)(ws + WS_TOK + (size_t)b * 128) + (lane & 7);
        gu32* inbox = (gu32*)(ws + WS_FLAG + (size_t)(b * 8 + hkv) * 128) + (lane & 7);
        gu32* outbox = (gu32*)(ws + WS_FLAG + (size_t)(b * 8 + (lane & 7)) * 128) + hkv;
        tok = __hip_atomic_load(tokp, RLX_AGENT);
        const unsigned mytok = __builtin_amdgcn_readlane(tok, hkv);
        barrier_lgkm();
        if (lane < 8) __hip_atomic_store(outbox, tok, RLX_AGENT);
        for (unsigned spin = 0; spin < (1u << 20); ++spin) {
          const unsigned f = __hip_atomic_load(inbox, RLX_AGENT);
          const unsigned t2 = __hip_atomic_load(tokp, RLX_AGENT);
          if (lane < 8 && t2 != tok) { tok = t2; __hip_atomic_store(outbox, tok, RLX_AGENT); }
          if (__all(f == mytok)) break;
          __builtin_amdgcn_s_sleep(1);
        }
        asm volatile("" ::: "memory");
      } else {
        barrier_lgkm();
      }
    } else {
      barrier_lgkm();
#pragma unroll
      for (int t = 0; t < 8; ++t) { acc[t][0] += *(const f32x4*)(ox + (2 * t) * 1024); acc[t][1] += *(const f32x4*)(ox + (2 * t + 1) * 1024); }
      const unsigned off = (unsigned)(((b * 8 + 2 * w4 + (g >> 1)) * 8 + hkv) * 4096 + (lane & 31) * 16);
#pragma unroll
      for (int qt = 0; qt < 2; ++qt)
#pragma unroll
        for (int i = 0; i < 4; ++i) {
          u32x4 v;
#pragma unroll
          for (int m = 0; m < 4; ++m) v[m] = cvtpk(acc[2 * m][qt][i], acc[2 * m + 1][qt][i]);
          __builtin_amdgcn_raw_buffer_store_b128(v, xr, off + (qt * 4 + i) * 512, 0, 16);
        }
      wait_vm<0>();
      STAMP(6);
      barrier_lgkm();
    }
  }
  barrier_lgkm();
  STAMP(7);
  {
    const unsigned off = (unsigned)(((b * 8 + hkv) * 8 + (lane >> 5)) * 4096 + wave * 512 + (lane & 31) * 16);
    u32x4 d[4];
#pragma unroll
    for (int p = 0; p < 4; ++p) d[p] = __builtin_amdgcn_raw_buffer_load_b128(xr, off + p * 8192, 0, 16);
    float sum[8];
#pragma unroll
    for (int m = 0; m < 4; ++m) {
      float lo = 0.f, hi = 0.f;
#pragma unroll
      for (int p = 0; p < 4; ++p) { lo += __uint_as_float(d[p][m] << 16); hi += __uint_as_float(d[p][m] & 0xffff0000u); }
      sum[2 * m] = lo; sum[2 * m + 1] = hi;
    }
#pragma unroll
    for (int e = 0; e < 8; ++e) {
      auto rr = __builtin_amdgcn_permlane32_swap(__float_as_uint(sum[e]), __float_as_uint(sum[e]), false, false);
      sum[e] = __uint_as_float(rr[0]) + __uint_as_float(rr[1]);
    }
    if (lane < 32) {
      u32x4 v;
#pragma unroll
      for (int m = 0; m < 4; ++m) v[m] = cvtpk(sum[2 * m], sum[2 * m + 1]);
      const int q = (lane & 15) + 16 * (wave >> 2), n = 64 * hkv + 32 * ((lane >> 4) & 1) + 8 * (wave & 3);
      *(u32x4*)(out + (size_t)b * (SQ * DM) + q * DM + n) = v;
    }
  }
  if (wave == 0) {
    gu32* tokp = (gu32*)(ws + WS_TOK + (size_t)b * 128) + (lane & 7);
    gu32* outbox = (gu32*)(ws + WS_FLAG + (size_t)(b * 8 + (lane & 7)) * 128) + hkv;
    const unsigned t2 = __hip_atomic_load(tokp, RLX_AGENT);
    if (lane < 8 && t2 != tok) __hip_atomic_store(outbox, t2, RLX_AGENT);
  }
  STAMP(8);
  STAMP_FLUSH;
}

extern "C" void kernel_launch(void* const* d_in, const int* in_sizes, int n_in, void* d_out, int out_size, void* d_ws,
                              size_t ws_size, hipStream_t stream) {
  const u16* x = (const u16*)d_in[0];
  const u16* kc = (const u16*)d_in[1];
  const u16* vc = (const u16*)d_in[2];
  const u16* wq = (const u16*)d_in[3];
  const u16* wo = (const u16*)d_in[4];
  (void)hipFuncSetAttribute((const void*)fused_kernel, hipFuncAttributeMaxDynamicSharedMemorySize, LDS_TOTAL);
  fused_kernel<<<256, 512, LDS_TOTAL, stream>>>(x, kc, vc, wq, wo, (char*)d_ws, (u16*)d_out);
}
```

```cpp
#include <hip/hip_runtime.h>
#include <hip/hip_bf16.h>

typedef __attribute__((ext_vector_type(8))) short bf16x8;
typedef __attribute__((ext_vector_type(4))) short s16x4;
typedef __attribute__((ext_vector_type(4))) float f32x4;
typedef __attribute__((ext_vector_type(16))) float f32x16;
typedef __attribute__((ext_vector_type(4))) unsigned u32x4;
typedef __attribute__((ext_vector_type(2))) unsigned u32x2;
typedef unsigned short u16;

#define DI __device__ __forceinline__
#define LDS3 __attribute__((address_space(3)))

constexpr int B_ = 32, SQ = 32, DM = 512, HQ = 32, HKV = 8, SK = 512, DH = 128;
constexpr int LDS_RING = 0;
constexpr int LDS_X = 131072;
constexpr int LDS_TOTAL = 163840;

#define STAMP(i) do {} while (0)
#define STAMP_DECL
#define STAMP_FLUSH do {} while (0)

typedef __attribute__((ext_vector_type(2))) __bf16 bf16x2_t;
typedef __attribute__((ext_vector_type(2))) float f32x2;
DI unsigned cvtpk(float lo, float hi) {
  f32x2 v = {lo, hi};
  return __builtin_bit_cast(unsigned, __builtin_convertvector(v, bf16x2_t));
}

DI void barrier_lgkm() { asm volatile("s_waitcnt lgkmcnt(0)\n\ts_barrier" ::: "memory"); }
DI void barrier_raw() { asm volatile("s_barrier" ::: "memory"); }

DI void glds16(const void* sbase, unsigned voff, unsigned lds_dst) {
  unsigned keep;
  asm volatile("s_mov_b32 %0, m0\n\ts_mov_b32 m0, %3\n\ts_nop 0\n\tglobal_load_lds_dwordx4 %1, %2\n\ts_mov_b32 m0, %0"
               : "=&s"(keep) : "v"(voff), "s"(sbase), "s"(lds_dst) : "memory");
}
DI void glds16_nt(const void* sbase, unsigned voff, unsigned lds_dst) {
  unsigned keep;
  asm volatile("s_mov_b32 %0, m0\n\ts_mov_b32 m0, %3\n\ts_nop 0\n\tglobal_load_lds_dwordx4 %1, %2 nt\n\ts_mov_b32 m0, %0"
               : "=&s"(keep) : "v"(voff), "s"(sbase), "s"(lds_dst) : "memory");
}
DI void gload16(u32x4& dst, const void* sbase, unsigned voff) {
  asm volatile("global_load_dwordx4 %0, %1, %2" : "=v"(dst) : "v"(voff), "s"(sbase) : "memory");
}
template <int N>
DI void wait_raw(u32x4 (&r)[8]) {
  asm volatile("s_waitcnt vmcnt(%8)"
               : "+v"(r[0]), "+v"(r[1]), "+v"(r[2]), "+v"(r[3]), "+v"(r[4]), "+v"(r[5]), "+v"(r[6]), "+v"(r[7]) : "i"(N) : "memory");
}
template <int N> DI void wait_vm() { asm volatile("s_waitcnt vmcnt(%0)" :: "i"(N) : "memory"); }

template <int RS>
DI void wload(u32x4 (&raw)[8], const u16* p, unsigned voff, int ks) {
  const u16* q = p + (size_t)(32 * ks) * RS;
#pragma unroll
  for (int j = 0; j < 8; ++j) gload16(raw[j], q + (size_t)j * RS, voff);
}
DI void wstep(const u32x4 (&raw)[8], f32x4 (&acc)[8][2], const char* bimg, int ks, int r16, int g) {
  const unsigned boff = r16 * 1024 + (((4 * ks + g) ^ r16) << 4);
  const bf16x8 b0 = *(const bf16x8*)(bimg + boff);
  const bf16x8 b1 = *(const bf16x8*)(bimg + boff + 16384);
#pragma unroll
  for (int c = 0; c < 4; ++c) {
    u32x4 f0, f1;
#pragma unroll
    for (int m = 0; m < 4; ++m) {
      f0[m] = __builtin_amdgcn_perm(raw[2 * m + 1][c], raw[2 * m][c], 0x05040100u);
      f1[m] = __builtin_amdgcn_perm(raw[2 * m + 1][c], raw[2 * m][c], 0x07060302u);
    }
    const bf16x8 a0 = __builtin_bit_cast(bf16x8, f0), a1 = __builtin_bit_cast(bf16x8, f1);
    acc[2 * c][0] = __builtin_amdgcn_mfma_f32_16x16x32_bf16(a0, b0, acc[2 * c][0], 0, 0, 0);
    acc[2 * c][1] = __builtin_amdgcn_mfma_f32_16x16x32_bf16(a0, b1, acc[2 * c][1], 0, 0, 0);
    acc[2 * c + 1][0] = __builtin_amdgcn_mfma_f32_16x16x32_bf16(a1, b0, acc[2 * c + 1][0], 0, 0, 0);
    acc[2 * c + 1][1] = __builtin_amdgcn_mfma_f32_16x16x32_bf16(a1, b1, acc[2 * c + 1][1], 0, 0, 0);
  }
}
template <int RS, bool TRIG>
DI void proj_loop(u32x4 (&raw)[4][8], f32x4 (&acc)[8][2], const u16* wp, unsigned wv, const char* bimg, int rot, int r16, int g, int stag) {
#pragma unroll 1
  for (int it = 0; it < 3; ++it) {
#pragma unroll
    for (int u = 0; u < 4; ++u) {
      const int s = 4 * it + u;
      wait_raw<16>(raw[u]);
      wload<RS>(raw[(u + 3) & 3], wp, wv, (s + 3 + rot) & 15);
      __builtin_amdgcn_sched_barrier(0);
      wstep(raw[u], acc, bimg, (s + rot) & 15, r16, g);
      __builtin_amdgcn_sched_barrier(0);
      if (TRIG) { if (u == stag) barrier_raw(); }
    }
  }
  wait_raw<16>(raw[0]);
  wload<RS>(raw[3], wp, wv, (15 + rot) & 15);
  __builtin_amdgcn_sched_barrier(0);
  wstep(raw[0], acc, bimg, (12 + rot) & 15, r16, g);
  wait_raw<16>(raw[1]); wstep(raw[1], acc, bimg, (13 + rot) & 15, r16, g);
  wait_raw<8>(raw[2]);  wstep(raw[2], acc, bimg, (14 + rot) & 15, r16, g);
  wait_raw<0>(raw[3]);  wstep(raw[3], acc, bimg, (15 + rot) & 15, r16, g);
}

typedef __attribute__((address_space(1))) unsigned gu32;
#define RLX_AGENT __ATOMIC_RELAXED, __HIP_MEMORY_SCOPE_AGENT
constexpr size_t WS_XBUF = 0, XBUF_BYTES = (size_t)32 * 8 * 8 * 4096;
constexpr size_t WS_FLAG = XBUF_BYTES;
constexpr size_t WS_TOK = WS_FLAG + 256 * 128;

__global__ __launch_bounds__(512, 2) void fused_kernel(const u16* x, const u16* kc, const u16* vc, const u16* wq, const u16* wo,
                                                       char* ws, u16* out
) {
  extern __shared__ __attribute__((aligned(16))) char smem[];
  const int tid = threadIdx.x, lane = tid & 63;
  const int wave = __builtin_amdgcn_readfirstlane(tid >> 6);
  const int hkv = blockIdx.x & 7, b = blockIdx.x >> 3;
  const int w4 = wave & 3;
  const bool isW = wave >= 4;
  const int r16 = lane & 15, g = lane >> 4;
  const int r32 = lane & 31, h = lane >> 5;
  STAMP_DECL
  STAMP(0);
  const char* ximg = smem + LDS_X;
  const int stag = b & 3;
  const int rot = b & 15;
  unsigned tok = 0;
  const __amdgpu_buffer_rsrc_t xr = __builtin_amdgcn_make_buffer_rsrc(ws + WS_XBUF, 0, (int)XBUF_BYTES, 0x00020000);
  const unsigned sbase = (unsigned)(size_t)smem;
  const u16* kp = kc + (size_t)(b * HKV + hkv) * SK * DH;
  const u16* vp = vc + (size_t)(b * HKV + hkv) * SK * DH;
#define KV_BURST(t) do { \
    int ln_ = lane; asm volatile("" : "+v"(ln_)); \
    const int r_ = ln_ & 15, g_ = ln_ >> 4; \
    _Pragma("unroll") for (int j = 0; j < 4; ++j) { const int row = 16 * w4 + 4 * j + g_; \
      glds16_nt(kp + (t) * 64 * DH, (row * DH + 8 * (r_ ^ (row & 15))) * 2, sbase + LDS_RING + ((t) & 3) * 32768 + (16 * w4 + 4 * j) * 256); } \
    _Pragma("unroll") for (int j = 0; j < 4; ++j) { const int row = 16 * w4 + 4 * j + g_; \
      glds16_nt(vp + (t) * 64 * DH, (row * DH + 8 * (r_ ^ (((row & 3) << 2) | ((row >> 2) & 3)))) * 2, sbase + LDS_RING + ((t) & 3) * 32768 + 16384 + (16 * w4 + 4 * j) * 256); } \
  } while (0)

  if (!isW) {
    {
      const u16* xp = x + (size_t)b * SQ * DM;
#pragma unroll
      for (int i = 0; i < 8; ++i) {
        const int row = 8 * w4 + i;
        const int c = (lane & ~15) | ((lane & 15) ^ (row & 15));
        glds16(xp, (unsigned)(row * DM + c * 8) * 2, sbase + LDS_X + row * 1024);
      }
    }
    bf16x8 qf[8];
    {
      const u16* wp = wq + hkv * 512 + w4 * 128;
      const unsigned wv = (unsigned)(8 * g * 4096 + r16 * 8) * 2;
      u32x4 raw[4][8];
      wload<4096>(raw[0], wp, wv, (0 + rot) & 15);
      f32x4 acc[8][2];
#pragma unroll
      for (int t = 0; t < 8; ++t) { acc[t][0] = f32x4{0, 0, 0, 0}; acc[t][1] = f32x4{0, 0, 0, 0}; }
      wait_vm<8>();
      barrier_lgkm();
      wload<4096>(raw[1], wp, wv, (1 + rot) & 15);
      wload<4096>(raw[2], wp, wv, (2 + rot) & 15);
      STAMP(1);
      proj_loop<4096, true>(raw, acc, wp, wv, ximg, rot, r16, g, stag);
#pragma unroll
      for (int i = 0; i < 4; ++i) {
        u32x4 a, bb;
#pragma unroll
        for (int m = 0; m < 4; ++m) {
          unsigned a_ = cvtpk(acc[2 * m][0][i], acc[2 * m + 1][0][i]);
          unsigned b_ = cvtpk(acc[2 * m][1][i], acc[2 * m + 1][1][i]);
          auto r = __builtin_amdgcn_permlane16_swap(a_, b_, false, false);
          a[m] = r[0]; bb[m] = r[1];
        }
        qf[i] = __builtin_bit_cast(bf16x8, a);
        qf[4 + i] = __builtin_bit_cast(bf16x8, bb);
      }
    }
    STAMP(2);
    unsigned kaddr[8], vaddr[8];
#pragma unroll
    for (int s = 0; s < 8; ++s) kaddr[s] = r32 * 256 + 16 * ((8 * h + s) ^ (r32 & 15));
    {
      const int blk = (lane >> 4) & 1, qq = (lane & 15) >> 2, p = lane & 3;
#pragma unroll
      for (int dt = 0; dt < 4; ++dt)
#pragma unroll
        for (int e = 0; e < 2; ++e)
          vaddr[dt * 2 + e] = (8 * e + 4 * h + qq) * 256 + 16 * (4 * (dt ^ qq) + ((2 * blk + (p >> 1)) ^ (2 * e + h))) + 8 * (p & 1) + 16384;
    }
    const float csc = 0.08838834764831845f * 1.4426950408889634f;
    float m_run = -__builtin_inff(), l_run = 0.f;
    f32x16 o[4];
#pragma unroll
    for (int dt = 0; dt < 4; ++dt)
#pragma unroll
      for (int i = 0; i < 16; ++i) o[dt][i] = 0.f;

    f32x16 lacc;
#pragma unroll
    for (int i = 0; i < 16; ++i) lacc[i] = 0.f;
    bf16x8 ones;
#pragma unroll
    for (int i = 0; i < 8; ++i) ones[i] = (short)0x3F80;
#pragma unroll 1
    for (int j = 0; j < 4; ++j) {
      barrier_lgkm();
      const char* slot = smem + LDS_RING + (j & 1) * 65536;
      f32x16 sc[4];
      bf16x8 kf[2][4];
      float mxa = m_run, mxb = m_run;
#define K_LOAD(gi, buf) do { _Pragma("unroll") for (int s_ = 0; s_ < 4; ++s_) \
        kf[buf][s_] = *(const bf16x8*)(slot + kaddr[2 * ((gi) & 3) + (s_ >> 1)] + (s_ & 1) * 8192 + ((gi) >> 2) * 32768); } while (0)
      K_LOAD(0, 0);
#pragma unroll
      for (int gi = 0; gi < 8; ++gi) {
        const int pr = gi >> 2;
        __builtin_amdgcn_sched_barrier(0);
        if (gi < 7) K_LOAD(gi + 1, (gi + 1) & 1);
        if ((gi & 3) == 0) {
#pragma unroll
          for (int i = 0; i < 16; ++i) { sc[2 * pr][i] = 0.f; sc[2 * pr + 1][i] = 0.f; }
        }
#pragma unroll
        for (int s = 0; s < 4; ++s)
          sc[2 * pr + (s & 1)] = __builtin_amdgcn_mfma_f32_32x32x16_bf16(kf[gi & 1][s], qf[2 * (gi & 3) + (s >> 1)], sc[2 * pr + (s & 1)], 0, 0, 0);
        if (gi == 5 || gi == 6) {
#pragma unroll
          for (int i = 0; i < 8; ++i) { mxa = fmaxf(mxa, sc[gi - 5][i]); mxb = fmaxf(mxb, sc[gi - 5][8 + i]); }
        }
      }
      bf16x8 pf[2], vf[2][4];
#define V_LOAD(ks, buf) do { \
        const char* vb_ = slot + ((ks) >> 2) * 32768 + 4096 * ((ks) & 3); \
        _Pragma("unroll") for (int dt = 0; dt < 4; ++dt) { \
          const s16x4 lo = __builtin_amdgcn_ds_read_tr16_b64_v4i16((LDS3 s16x4*)(vb_ + vaddr[dt * 2 + 0])); \
          const s16x4 hi = __builtin_amdgcn_ds_read_tr16_b64_v4i16((LDS3 s16x4*)(vb_ + vaddr[dt * 2 + 1])); \
          vf[buf][dt] = __builtin_shufflevector(lo, hi, 0, 1, 2, 3, 4, 5, 6, 7); } } while (0)
#define P_PREP(ks, buf) do { \
        const int u_ = (ks) >> 1, r0_ = 8 * ((ks) & 1); u32x4 pw_; \
        _Pragma("unroll") for (int m = 0; m < 4; ++m) \
          pw_[m] = cvtpk(__builtin_amdgcn_exp2f(fmaf(sc[u_][r0_ + 2 * m], csc, nm)), __builtin_amdgcn_exp2f(fmaf(sc[u_][r0_ + 2 * m + 1], csc, nm))); \
        pf[buf] = __builtin_bit_cast(bf16x8, pw_); } while (0)
      __builtin_amdgcn_sched_barrier(0);
      V_LOAD(0, 0);
#pragma unroll
      for (int i = 0; i < 16; ++i) { mxa = fmaxf(mxa, sc[2][i]); mxb = fmaxf(mxb, sc[3][i]); }
      float mx = fmaxf(mxa, mxb);
      {
        auto rr = __builtin_amdgcn_permlane32_swap(__float_as_uint(mx), __float_as_uint(mx), false, false);
        mx = fmaxf(__uint_as_float(rr[0]), __uint_as_float(rr[1]));
      }
      const float alpha = __builtin_amdgcn_exp2f((m_run - mx) * csc);
      const float nm = -mx * csc;
      m_run = mx;
      P_PREP(0, 0);
      lacc[0] *= alpha;
#pragma unroll
      for (int ks = 0; ks < 8; ++ks) {
        __builtin_amdgcn_sched_barrier(0);
        if (ks < 7) { V_LOAD(ks + 1, (ks + 1) & 1); }
        lacc = __builtin_amdgcn_mfma_f32_32x32x16_bf16(ones, pf[ks & 1], lacc, 0, 0, 0);
#pragma unroll
        for (int dt = 0; dt < 4; ++dt) {
          if (ks == 0) {
#pragma unroll
            for (int i = 0; i < 16; ++i) o[dt][i] *= alpha;
          }
          o[dt] = __builtin_amdgcn_mfma_f32_32x32x16_bf16(vf[ks & 1][dt], pf[ks & 1], o[dt], 0, 0, 0);
        }
        if (ks < 7) { P_PREP(ks + 1, (ks + 1) & 1); }
      }
    }
    l_run = lacc[0];
    STAMP(3);
    const float inv = 1.0f / l_run;
#pragma unroll
    for (int dt = 0; dt < 4; ++dt)
#pragma unroll
      for (int a = 0; a < 4; ++a) {
        u32x2 v;
        v[0] = cvtpk(o[dt][4 * a] * inv, o[dt][4 * a + 1] * inv);
        v[1] = cvtpk(o[dt][4 * a + 2] * inv, o[dt][4 * a + 3] * inv);
        const int c = 16 * w4 + 4 * dt + a;
        *(u32x2*)(smem + LDS_X + r32 * 1024 + 16 * (c ^ (r32 & 15)) + 8 * h) = v;
      }
    barrier_lgkm();
    STAMP(4);
    if (wave == 0) {
      gu32* tokp = (gu32*)(ws + WS_TOK + (size_t)b * 128) + (lane & 7);
      gu32* inbox = (gu32*)(ws + WS_FLAG + (size_t)(b * 8 + hkv) * 128) + (lane & 7);
      gu32* outbox = (gu32*)(ws + WS_FLAG + (size_t)(b * 8 + (lane & 7)) * 128) + hkv;
      tok = __hip_atomic_load(tokp, RLX_AGENT);
      const unsigned mytok = __builtin_amdgcn_readlane(tok, hkv);
      barrier_lgkm();
      if (lane < 8) __hip_atomic_store(outbox, tok, RLX_AGENT);
      for (unsigned spin = 0; spin < (1u << 20); ++spin) {
        const unsigned f = __hip_atomic_load(inbox, RLX_AGENT);
        const unsigned t2 = __hip_atomic_load(tokp, RLX_AGENT);
        if (lane < 8 && t2 != tok) { tok = t2; __hip_atomic_store(outbox, tok, RLX_AGENT); }
        if (__all(f == mytok)) break;
        __builtin_amdgcn_s_sleep(1);
      }
      asm volatile("" ::: "memory");
    } else {
      barrier_lgkm();
    }
  } else {
    barrier_lgkm();
    STAMP(1);
    if (wave == 4) {
      gu32* inbox = (gu32*)(ws + WS_FLAG + (size_t)(b * 8 + hkv) * 128);
      gu32* mytokp = (gu32*)(ws + WS_TOK + (size_t)b * 128) + hkv;
      const unsigned v = __hip_atomic_load(lane < 8 ? inbox + lane : mytokp, RLX_AGENT);
      unsigned t = __builtin_amdgcn_readlane(v, 8) + 1;
      while (__any(v == t)) ++t;
      if (lane == 0) __hip_atomic_store(mytokp, t, RLX_AGENT);
    }
#pragma unroll 1
    for (int t = 0; t < 3; ++t) { KV_BURST(t); barrier_raw(); }
    KV_BURST(3);
    const u16* wp = wo + (size_t)(hkv * 512) * 512 + w4 * 128;
    const unsigned wv = (unsigned)(8 * g * 512 + r16 * 8) * 2;
    u32x4 raw[4][8];
    wait_vm<16>();
    barrier_lgkm();
    wload<512>(raw[0], wp, wv, (0 + rot) & 15);
    wload<512>(raw[1], wp, wv, (1 + rot) & 15);
    wait_vm<16>();
    barrier_lgkm();
    KV_BURST(4);
    KV_BURST(5);
    wload<512>(raw[2], wp, wv, (2 + rot) & 15);
    wait_vm<8>();
    barrier_lgkm();
    KV_BURST(6);
    KV_BURST(7);
    wait_vm<0>();
    barrier_lgkm();
    f32x4 acc[8][2];
#pragma unroll
    for (int t = 0; t < 8; ++t) { acc[t][0] = f32x4{0, 0, 0, 0}; acc[t][1] = f32x4{0, 0, 0, 0}; }
    STAMP(3);
    barrier_lgkm();
    STAMP(4);
    proj_loop<512, false>(raw, acc, wp, wv, ximg, rot, r16, g, 0);
    STAMP(5);
    {
      const unsigned off = (unsigned)(((b * 8 + 2 * w4 + (g >> 1)) * 8 + hkv) * 4096 + (lane & 31) * 16);
#pragma unroll
      for (int qt = 0; qt < 2; ++qt)
#pragma unroll
        for (int i = 0; i < 4; ++i) {
          u32x4 v;
#pragma unroll
          for (int m = 0; m < 4; ++m) v[m] = cvtpk(acc[2 * m][qt][i], acc[2 * m + 1][qt][i]);
          __builtin_amdgcn_raw_buffer_store_b128(v, xr, off + (qt * 4 + i) * 512, 0, 16);
        }
    }
    wait_vm<0>();
    STAMP(6);
    barrier_lgkm();
  }
  barrier_lgkm();
  STAMP(7);
  {
    const unsigned off = (unsigned)(((b * 8 + hkv) * 8 + (lane >> 5)) * 4096 + wave * 512 + (lane & 31) * 16);
    u32x4 d[4];
#pragma unroll
    for (int p = 0; p < 4; ++p) d[p] = __builtin_amdgcn_raw_buffer_load_b128(xr, off + p * 8192, 0, 16);
    float sum[8];
#pragma unroll
    for (int m = 0; m < 4; ++m) {
      float lo = 0.f, hi = 0.f;
#pragma unroll
      for (int p = 0; p < 4; ++p) { lo += __uint_as_float(d[p][m] << 16); hi += __uint_as_float(d[p][m] & 0xffff0000u); }
      sum[2 * m] = lo; sum[2 * m + 1] = hi;
    }
#pragma unroll
    for (int e = 0; e < 8; ++e) {
      auto rr = __builtin_amdgcn_permlane32_swap(__float_as_uint(sum[e]), __float_as_uint(sum[e]), false, false);
      sum[e] = __uint_as_float(rr[0]) + __uint_as_float(rr[1]);
    }
    if (lane < 32) {
      u32x4 v;
#pragma unroll
      for (int m = 0; m < 4; ++m) v[m] = cvtpk(sum[2 * m], sum[2 * m + 1]);
      const int q = (lane & 15) + 16 * (wave >> 2), n = 64 * hkv + 32 * ((lane >> 4) & 1) + 8 * (wave & 3);
      *(u32x4*)(out + (size_t)b * (SQ * DM) + q * DM + n) = v;
    }
  }
  if (wave == 0) {
    gu32* tokp = (gu32*)(ws + WS_TOK + (size_t)b * 128) + (lane & 7);
    gu32* outbox = (gu32*)(ws + WS_FLAG + (size_t)(b * 8 + (lane & 7)) * 128) + hkv;
    const unsigned t2 = __hip_atomic_load(tokp, RLX_AGENT);
    if (lane < 8 && t2 != tok) __hip_atomic_store(outbox, t2, RLX_AGENT);
  }
  STAMP(8);
  STAMP_FLUSH;
}

extern "C" void kernel_launch(void* const* d_in, const int* in_sizes, int n_in, void* d_out, int out_size, void* d_ws,
                              size_t ws_size, hipStream_t stream) {
  const u16* x = (const u16*)d_in[0];
  const u16* kc = (const u16*)d_in[1];
  const u16* vc = (const u16*)d_in[2];
  const u16* wq = (const u16*)d_in[3];
  const u16* wo = (const u16*)d_in[4];
  (void)hipFuncSetAttribute((const void*)fused_kernel, hipFuncAttributeMaxDynamicSharedMemorySize, LDS_TOTAL);
  fused_kernel<<<256, 512, LDS_TOTAL, stream>>>(x, kc, vc, wq, wo, (char*)d_ws, (u16*)d_out);
}
```

```cpp
#include <hip/hip_runtime.h>
#include <hip/hip_bf16.h>

typedef __attribute__((ext_vector_type(8))) short bf16x8;
typedef __attribute__((ext_vector_type(4))) short s16x4;
typedef __attribute__((ext_vector_type(4))) float f32x4;
typedef __attribute__((ext_vector_type(16))) float f32x16;
typedef __attribute__((ext_vector_type(4))) unsigned u32x4;
typedef __attribute__((ext_vector_type(2))) unsigned u32x2;
typedef unsigned short u16;

#define DI __device__ __forceinline__
#define LDS3 __attribute__((address_space(3)))

constexpr int B_ = 32, SQ = 32, DM = 512, HQ = 32, HKV = 8, SK = 512, DH = 128;
constexpr int LDS_RING = 0;
constexpr int LDS_X = 131072;
constexpr int LDS_TOTAL = 163840;
constexpr int LDS_OIMG = LDS_RING + 65536;

#define STAMP(i) do {} while (0)
#define STAMP_DECL
#define STAMP_FLUSH do {} while (0)

typedef __attribute__((ext_vector_type(2))) __bf16 bf16x2_t;
typedef __attribute__((ext_vector_type(2))) float f32x2;
DI unsigned cvtpk(float lo, float hi) {
  f32x2 v = {lo, hi};
  return __builtin_bit_cast(unsigned, __builtin_convertvector(v, bf16x2_t));
}

DI void barrier_lgkm() { asm volatile("s_waitcnt lgkmcnt(0)\n\ts_barrier" ::: "memory"); }
DI void barrier_raw() { asm volatile("s_barrier" ::: "memory"); }

DI void glds16(const void* sbase, unsigned voff, unsigned lds_dst) {
  unsigned keep;
  asm volatile("s_mov_b32 %0, m0\n\ts_mov_b32 m0, %3\n\ts_nop 0\n\tglobal_load_lds_dwordx4 %1, %2\n\ts_mov_b32 m0, %0"
               : "=&s"(keep) : "v"(voff), "s"(sbase), "s"(lds_dst) : "memory");
}
DI void glds16_nt(const void* sbase, unsigned voff, unsigned lds_dst) {
  unsigned keep;
  asm volatile("s_mov_b32 %0, m0\n\ts_mov_b32 m0, %3\n\ts_nop 0\n\tglobal_load_lds_dwordx4 %1, %2 nt\n\ts_mov_b32 m0, %0"
               : "=&s"(keep) : "v"(voff), "s"(sbase), "s"(lds_dst) : "memory");
}
DI void gload16(u32x4& dst, const void* sbase, unsigned voff) {
  asm volatile("global_load_dwordx4 %0, %1, %2" : "=v"(dst) : "v"(voff), "s"(sbase) : "memory");
}
template <int N>
DI void wait_raw(u32x4 (&r)[8]) {
  asm volatile("s_waitcnt vmcnt(%8)"
               : "+v"(r[0]), "+v"(r[1]), "+v"(r[2]), "+v"(r[3]), "+v"(r[4]), "+v"(r[5]), "+v"(r[6]), "+v"(r[7]) : "i"(N) : "memory");
}
template <int N> DI void wait_vm() { asm volatile("s_waitcnt vmcnt(%0)" :: "i"(N) : "memory"); }

template <int RS>
DI void wload(u32x4 (&raw)[8], const u16* p, unsigned voff, int ks) {
  const u16* q = p + (size_t)(32 * ks) * RS;
#pragma unroll
  for (int j = 0; j < 8; ++j) gload16(raw[j], q + (size_t)j * RS, voff);
}
template <int QOFF = 16384>
DI void wstep(const u32x4 (&raw)[8], f32x4 (&acc)[8][2], const char* bimg, int ks, int r16, int g) {
  const unsigned boff = r16 * 1024 + (((4 * ks + g) ^ r16) << 4);
  const bf16x8 b0 = *(const bf16x8*)(bimg + boff);
  const bf16x8 b1 = *(const bf16x8*)(bimg + boff + QOFF);
#pragma unroll
  for (int c = 0; c < 4; ++c) {
    u32x4 f0, f1;
#pragma unroll
    for (int m = 0; m < 4; ++m) {
      f0[m] = __builtin_amdgcn_perm(raw[2 * m + 1][c], raw[2 * m][c], 0x05040100u);
      f1[m] = __builtin_amdgcn_perm(raw[2 * m + 1][c], raw[2 * m][c], 0x07060302u);
    }
    const bf16x8 a0 = __builtin_bit_cast(bf16x8, f0), a1 = __builtin_bit_cast(bf16x8, f1);
    acc[2 * c][0] = __builtin_amdgcn_mfma_f32_16x16x32_bf16(a0, b0, acc[2 * c][0], 0, 0, 0);
    acc[2 * c][1] = __builtin_amdgcn_mfma_f32_16x16x32_bf16(a0, b1, acc[2 * c][1], 0, 0, 0);
    acc[2 * c + 1][0] = __builtin_amdgcn_mfma_f32_16x16x32_bf16(a1, b0, acc[2 * c + 1][0], 0, 0, 0);
    acc[2 * c + 1][1] = __builtin_amdgcn_mfma_f32_16x16x32_bf16(a1, b1, acc[2 * c + 1][1], 0, 0, 0);
  }
}
template <int RS, bool TRIG>
DI void proj_loop(u32x4 (&raw)[4][8], f32x4 (&acc)[8][2], const u16* wp, unsigned wv, const char* bimg, int rot, int r16, int g, int stag) {
#pragma unroll 1
  for (int it = 0; it < 3; ++it) {
#pragma unroll
    for (int u = 0; u < 4; ++u) {
      const int s = 4 * it + u;
      wait_raw<16>(raw[u]);
      wload<RS>(raw[(u + 3) & 3], wp, wv, (s + 3 + rot) & 15);
      __builtin_amdgcn_sched_barrier(0);
      wstep(raw[u], acc, bimg, (s + rot) & 15, r16, g);
      __builtin_amdgcn_sched_barrier(0);
      if (TRIG) { if (u == stag) barrier_raw(); }
    }
  }
  wait_raw<16>(raw[0]);
  wload<RS>(raw[3], wp, wv, (15 + rot) & 15);
  __builtin_amdgcn_sched_barrier(0);
  wstep(raw[0], acc, bimg, (12 + rot) & 15, r16, g);
  wait_raw<16>(raw[1]); wstep(raw[1], acc, bimg, (13 + rot) & 15, r16, g);
  wait_raw<8>(raw[2]);  wstep(raw[2], acc, bimg, (14 + rot) & 15, r16, g);
  wait_raw<0>(raw[3]);  wstep(raw[3], acc, bimg, (15 + rot) & 15, r16, g);
}

typedef __attribute__((address_space(1))) unsigned gu32;
#define RLX_AGENT __ATOMIC_RELAXED, __HIP_MEMORY_SCOPE_AGENT
constexpr size_t WS_XBUF = 0, XBUF_BYTES = (size_t)32 * 8 * 8 * 4096;
constexpr size_t WS_FLAG = XBUF_BYTES;
constexpr size_t WS_TOK = WS_FLAG + 256 * 128;

__global__ __launch_bounds__(512, 2) void fused_kernel(const u16* x, const u16* kc, const u16* vc, const u16* wq, const u16* wo,
                                                       char* ws, u16* out
) {
  extern __shared__ __attribute__((aligned(16))) char smem[];
  const int tid = threadIdx.x, lane = tid & 63;
  const int wave = __builtin_amdgcn_readfirstlane(tid >> 6);
  const int hkv = blockIdx.x & 7, b = blockIdx.x >> 3;
  const int w4 = wave & 3;
  const bool isW = wave >= 4;
  const int r16 = lane & 15, g = lane >> 4;
  const int r32 = lane & 31, h = lane >> 5;
  STAMP_DECL
  STAMP(0);
  const char* ximg = smem + LDS_X;
  const int stag = b & 3;
  const int rot = b & 15;
  unsigned tok = 0;
  const __amdgpu_buffer_rsrc_t xr = __builtin_amdgcn_make_buffer_rsrc(ws + WS_XBUF, 0, (int)XBUF_BYTES, 0x00020000);
  const unsigned sbase = (unsigned)(size_t)smem;
  const u16* kp = kc + (size_t)(b * HKV + hkv) * SK * DH;
  const u16* vp = vc + (size_t)(b * HKV + hkv) * SK * DH;
#define K_ONE(t) do { \
    int ln_ = lane; asm volatile("" : "+v"(ln_)); \
    const int r_ = ln_ & 15, g_ = ln_ >> 4; \
    _Pragma("unroll") for (int j = 0; j < 4; ++j) { const int row = 16 * w4 + 4 * j + g_; \
      glds16_nt(kp + (t) * 64 * DH, (row * DH + 8 * (r_ ^ (row & 15))) * 2, sbase + LDS_RING + ((t) & 3) * 32768 + (16 * w4 + 4 * j) * 256); } \
  } while (0)
#define V_ONE(t) do { \
    int ln_ = lane; asm volatile("" : "+v"(ln_)); \
    const int r_ = ln_ & 15, g_ = ln_ >> 4; \
    _Pragma("unroll") for (int j = 0; j < 4; ++j) { const int row = 16 * w4 + 4 * j + g_; \
      glds16_nt(vp + (t) * 64 * DH, (row * DH + 8 * (r_ ^ (((row & 3) << 2) | ((row >> 2) & 3)))) * 2, sbase + LDS_RING + ((t) & 3) * 32768 + 16384 + (16 * w4 + 4 * j) * 256); } \
  } while (0)
#define KP(j) do { K_ONE(2 * (j)); K_ONE(2 * (j) + 1); } while (0)
#define VP(j) do { V_ONE(2 * (j)); V_ONE(2 * (j) + 1); } while (0)

  if (!isW) {
    {
      const u16* xp = x + (size_t)b * SQ * DM;
#pragma unroll
      for (int i = 0; i < 8; ++i) {
        const int row = 8 * w4 + i;
        const int c = (lane & ~15) | ((lane & 15) ^ (row & 15));
        glds16(xp, (unsigned)(row * DM + c * 8) * 2, sbase + LDS_X + row * 1024);
      }
    }
    bf16x8 qf[8];
    {
      const u16* wp = wq + hkv * 512 + w4 * 128;
      const unsigned wv = (unsigned)(8 * g * 4096 + r16 * 8) * 2;
      u32x4 raw[4][8];
      wload<4096>(raw[0], wp, wv, (0 + rot) & 15);
      f32x4 acc[8][2];
#pragma unroll
      for (int t = 0; t < 8; ++t) { acc[t][0] = f32x4{0, 0, 0, 0}; acc[t][1] = f32x4{0, 0, 0, 0}; }
      wait_vm<8>();
      barrier_lgkm();
      wload<4096>(raw[1], wp, wv, (1 + rot) & 15);
      wload<4096>(raw[2], wp, wv, (2 + rot) & 15);
      STAMP(1);
      proj_loop<4096, true>(raw, acc, wp, wv, ximg, rot, r16, g, stag);
#pragma unroll
      for (int i = 0; i < 4; ++i) {
        u32x4 a, bb;
#pragma unroll
        for (int m = 0; m < 4; ++m) {
          unsigned a_ = cvtpk(acc[2 * m][0][i], acc[2 * m + 1][0][i]);
          unsigned b_ = cvtpk(acc[2 * m][1][i], acc[2 * m + 1][1][i]);
          auto r = __builtin_amdgcn_permlane16_swap(a_, b_, false, false);
          a[m] = r[0]; bb[m] = r[1];
        }
        qf[i] = __builtin_bit_cast(bf16x8, a);
        qf[4 + i] = __builtin_bit_cast(bf16x8, bb);
      }
    }
    STAMP(2);
    unsigned kaddr[8], vaddr[8];
#pragma unroll
    for (int s = 0; s < 8; ++s) kaddr[s] = r32 * 256 + 16 * ((8 * h + s) ^ (r32 & 15));
    {
      const int blk = (lane >> 4) & 1, qq = (lane & 15) >> 2, p = lane & 3;
#pragma unroll
      for (int dt = 0; dt < 4; ++dt)
#pragma unroll
        for (int e = 0; e < 2; ++e)
          vaddr[dt * 2 + e] = (8 * e + 4 * h + qq) * 256 + 16 * (4 * (dt ^ qq) + ((2 * blk + (p >> 1)) ^ (2 * e + h))) + 8 * (p & 1) + 16384;
    }
    const float csc = 0.08838834764831845f * 1.4426950408889634f;
    float m_run = -__builtin_inff(), l_run = 0.f;
    f32x16 o[4];
#pragma unroll
    for (int dt = 0; dt < 4; ++dt)
#pragma unroll
      for (int i = 0; i < 16; ++i) o[dt][i] = 0.f;

    f32x16 lacc;
#pragma unroll
    for (int i = 0; i < 16; ++i) lacc[i] = 0.f;
    bf16x8 ones;
#pragma unroll
    for (int i = 0; i < 8; ++i) ones[i] = (short)0x3F80;
#pragma unroll 1
    for (int j = 0; j < 4; ++j) {
      barrier_lgkm();
      const char* slot = smem + LDS_RING + (j & 1) * 65536;
      f32x16 sc[4];
      bf16x8 kf[2][4];
      float mxa = m_run, mxb = m_run;
#define K_LOAD(gi, buf) do { _Pragma("unroll") for (int s_ = 0; s_ < 4; ++s_) \
        kf[buf][s_] = *(const bf16x8*)(slot + kaddr[2 * ((gi) & 3) + (s_ >> 1)] + (s_ & 1) * 8192 + ((gi) >> 2) * 32768); } while (0)
      K_LOAD(0, 0);
#pragma unroll
      for (int gi = 0; gi < 8; ++gi) {
        const int pr = gi >> 2;
        __builtin_amdgcn_sched_barrier(0);
        if (gi < 7) K_LOAD(gi + 1, (gi + 1) & 1);
        if ((gi & 3) == 0) {
#pragma unroll
          for (int i = 0; i < 16; ++i) { sc[2 * pr][i] = 0.f; sc[2 * pr + 1][i] = 0.f; }
        }
#pragma unroll
        for (int s = 0; s < 4; ++s)
          sc[2 * pr + (s & 1)] = __builtin_amdgcn_mfma_f32_32x32x16_bf16(kf[gi & 1][s], qf[2 * (gi & 3) + (s >> 1)], sc[2 * pr + (s & 1)], 0, 0, 0);
        if (gi == 5 || gi == 6) {
#pragma unroll
          for (int i = 0; i < 8; ++i) { mxa = fmaxf(mxa, sc[gi - 5][i]); mxb = fmaxf(mxb, sc[gi - 5][8 + i]); }
        }
      }
      bf16x8 pf[2], vf[2][4];
#define V_LOAD(ks, buf) do { \
        const char* vb_ = slot + ((ks) >> 2) * 32768 + 4096 * ((ks) & 3); \
        _Pragma("unroll") for (int dt = 0; dt < 4; ++dt) { \
          const s16x4 lo = __builtin_amdgcn_ds_read_tr16_b64_v4i16((LDS3 s16x4*)(vb_ + vaddr[dt * 2 + 0])); \
          const s16x4 hi = __builtin_amdgcn_ds_read_tr16_b64_v4i16((LDS3 s16x4*)(vb_ + vaddr[dt * 2 + 1])); \
          vf[buf][dt] = __builtin_shufflevector(lo, hi, 0, 1, 2, 3, 4, 5, 6, 7); } } while (0)
#define P_FMA(ks) do { const int u_ = (ks) >> 1, r0_ = 8 * ((ks) & 1); \
        _Pragma("unroll") for (int m = 0; m < 8; ++m) sc[u_][r0_ + m] = fmaf(sc[u_][r0_ + m], csc, nm); } while (0)
#define P_EXP(ks, buf) do { \
        const int u_ = (ks) >> 1, r0_ = 8 * ((ks) & 1); u32x4 pw_; \
        _Pragma("unroll") for (int m = 0; m < 4; ++m) \
          pw_[m] = cvtpk(__builtin_amdgcn_exp2f(sc[u_][r0_ + 2 * m]), __builtin_amdgcn_exp2f(sc[u_][r0_ + 2 * m + 1])); \
        pf[buf] = __builtin_bit_cast(bf16x8, pw_); } while (0)
      barrier_lgkm();
      V_LOAD(0, 0);
#pragma unroll
      for (int i = 0; i < 16; ++i) { mxa = fmaxf(mxa, sc[2][i]); mxb = fmaxf(mxb, sc[3][i]); }
      float mx = fmaxf(mxa, mxb);
      {
        auto rr = __builtin_amdgcn_permlane32_swap(__float_as_uint(mx), __float_as_uint(mx), false, false);
        mx = fmaxf(__uint_as_float(rr[0]), __uint_as_float(rr[1]));
      }
      const float alpha = __builtin_amdgcn_exp2f((m_run - mx) * csc);
      const float nm = -mx * csc;
      m_run = mx;
      P_FMA(0);
      P_FMA(1);
      P_EXP(0, 0);
      lacc[0] *= alpha;
#pragma unroll
      for (int ks = 0; ks < 8; ++ks) {
        __builtin_amdgcn_sched_barrier(0);
        if (ks < 7) { V_LOAD(ks + 1, (ks + 1) & 1); }
        lacc = __builtin_amdgcn_mfma_f32_32x32x16_bf16(ones, pf[ks & 1], lacc, 0, 0, 0);
#pragma unroll
        for (int dt = 0; dt < 4; ++dt) {
          if (ks == 0) {
#pragma unroll
            for (int i = 0; i < 16; ++i) o[dt][i] *= alpha;
          }
          o[dt] = __builtin_amdgcn_mfma_f32_32x32x16_bf16(vf[ks & 1][dt], pf[ks & 1], o[dt], 0, 0, 0);
        }
        if (ks < 7) { P_EXP(ks + 1, (ks + 1) & 1); }
        if (ks < 6) { P_FMA(ks + 2); }
        if (ks > 0 && ks < 6) {
#pragma unroll
          for (int gq = 0; gq < 4; ++gq) {
            __builtin_amdgcn_sched_group_barrier(0x008, 1, 0);
            __builtin_amdgcn_sched_group_barrier(0x100, 2, 0);
            __builtin_amdgcn_sched_group_barrier(0x400, 2, 0);
            __builtin_amdgcn_sched_group_barrier(0x002, 2, 0);
          }
          __builtin_amdgcn_sched_group_barrier(0x008, 1, 0);
          __builtin_amdgcn_sched_group_barrier(0x002, 4, 0);
        }
      }
    }
    l_run = lacc[0];
    STAMP(3);
    const float inv = 1.0f / l_run;
#pragma unroll
    for (int dt = 0; dt < 4; ++dt)
#pragma unroll
      for (int a = 0; a < 4; ++a) {
        u32x2 v;
        v[0] = cvtpk(o[dt][4 * a] * inv, o[dt][4 * a + 1] * inv);
        v[1] = cvtpk(o[dt][4 * a + 2] * inv, o[dt][4 * a + 3] * inv);
        const int c = 16 * w4 + 4 * dt + a;
        *(u32x2*)(smem + LDS_OIMG + (r32 & 15) * 1024 + (r32 >> 4) * 32768 + 16 * (c ^ (r32 & 15)) + 8 * h) = v;
      }
    barrier_lgkm();
    STAMP(4);
    if (wave == 0) {
      gu32* tokp = (gu32*)(ws + WS_TOK + (size_t)b * 128) + (lane & 7);
      gu32* inbox = (gu32*)(ws + WS_FLAG + (size_t)(b * 8 + hkv) * 128) + (lane & 7);
      gu32* outbox = (gu32*)(ws + WS_FLAG + (size_t)(b * 8 + (lane & 7)) * 128) + hkv;
      tok = __hip_atomic_load(tokp, RLX_AGENT);
      const unsigned mytok = __builtin_amdgcn_readlane(tok, hkv);
      barrier_lgkm();
      if (lane < 8) __hip_atomic_store(outbox, tok, RLX_AGENT);
      for (unsigned spin = 0; spin < (1u << 20); ++spin) {
        const unsigned f = __hip_atomic_load(inbox, RLX_AGENT);
        const unsigned t2 = __hip_atomic_load(tokp, RLX_AGENT);
        if (lane < 8 && t2 != tok) { tok = t2; __hip_atomic_store(outbox, tok, RLX_AGENT); }
        if (__all(f == mytok)) break;
        __builtin_amdgcn_s_sleep(1);
      }
      asm volatile("" ::: "memory");
    } else {
      barrier_lgkm();
    }
  } else {
    barrier_lgkm();
    STAMP(1);
    if (wave == 4) {
      gu32* inbox = (gu32*)(ws + WS_FLAG + (size_t)(b * 8 + hkv) * 128);
      gu32* mytokp = (gu32*)(ws + WS_TOK + (size_t)b * 128) + hkv;
      const unsigned v = __hip_atomic_load(lane < 8 ? inbox + lane : mytokp, RLX_AGENT);
      unsigned t = __builtin_amdgcn_readlane(v, 8) + 1;
      while (__any(v == t)) ++t;
      if (lane == 0) __hip_atomic_store(mytokp, t, RLX_AGENT);
    }
    const u16* wp = wo + (size_t)(hkv * 512) * 512 + w4 * 128;
    const unsigned wv = (unsigned)(8 * g * 512 + r16 * 8) * 2;
    u32x4 raw[4][8];
    const unsigned lst0 = sbase + LDS_X + w4 * 8192;
    const unsigned lst1 = sbase + LDS_RING + (w4 >> 1) * 32768 + (w4 & 1) * 8192;
    const unsigned lst2 = lst1 + 16384;
#define WO_LDS(idx, dst) do { const u16* q_ = wp + (size_t)(32 * (((idx) + rot) & 15)) * 512; \
      _Pragma("unroll") for (int j = 0; j < 8; ++j) glds16(q_ + j * 512, wv, (dst) + j * 1024); } while (0)
    barrier_raw();
    KP(0);
    barrier_raw();
    VP(0);
    barrier_raw();
    KP(1);
    wait_vm<16>();
    barrier_lgkm();
    VP(1);
    WO_LDS(4, lst0);
    wait_vm<24>();
    barrier_lgkm();
    KP(2);
    wload<512>(raw[0], wp, wv, (0 + rot) & 15);
    wait_vm<32>();
    barrier_lgkm();
    VP(2);
    wload<512>(raw[1], wp, wv, (1 + rot) & 15);
    wait_vm<40>();
    barrier_lgkm();
    KP(3);
    wload<512>(raw[2], wp, wv, (2 + rot) & 15);
    wait_vm<40>();
    barrier_lgkm();
    VP(3);
    wload<512>(raw[3], wp, wv, (3 + rot) & 15);
    wait_vm<40>();
    barrier_lgkm();
    WO_LDS(5, lst1);
    wait_vm<32>();
    barrier_lgkm();
    WO_LDS(6, lst2);
    wait_vm<24>();
    barrier_lgkm();
    f32x4 acc[8][2];
#pragma unroll
    for (int t = 0; t < 8; ++t) { acc[t][0] = f32x4{0, 0, 0, 0}; acc[t][1] = f32x4{0, 0, 0, 0}; }
    STAMP(3);
    wait_raw<0>(raw[0]); wait_raw<0>(raw[1]); wait_raw<0>(raw[2]); wait_raw<0>(raw[3]);
    barrier_lgkm();
    STAMP(4);
    {
      const char* oimg = smem + LDS_OIMG;
#pragma unroll
      for (int c = 0; c < 4; ++c) {
        wstep<32768>(raw[c], acc, oimg, (c + rot) & 15, r16, g);
        __builtin_amdgcn_sched_barrier(0);
        wload<512>(raw[c], wp, wv, (7 + c + rot) & 15);
      }
#pragma unroll
      for (int c = 0; c < 3; ++c) {
        const char* src = smem + (c == 0 ? LDS_X + w4 * 8192 : LDS_RING + (w4 >> 1) * 32768 + (w4 & 1) * 8192 + (c == 2 ? 16384 : 0)) + lane * 16;
        u32x4 tmp[8];
#pragma unroll
        for (int j = 0; j < 8; ++j) tmp[j] = *(const u32x4*)(src + j * 1024);
        wstep<32768>(tmp, acc, oimg, (4 + c + rot) & 15, r16, g);
        __builtin_amdgcn_sched_barrier(0);
      }
#pragma unroll
      for (int c = 7; c < 16; ++c) {
        const int bi = (c - 7) & 3;
        if (c <= 12) wait_raw<24>(raw[bi]); else if (c == 13) wait_raw<16>(raw[bi]); else if (c == 14) wait_raw<8>(raw[bi]); else wait_raw<0>(raw[bi]);
        wstep<32768>(raw[bi], acc, oimg, (c + rot) & 15, r16, g);
        __builtin_amdgcn_sched_barrier(0);
        if (c + 4 < 16) wload<512>(raw[bi], wp, wv, (c + 4 + rot) & 15);
      }
    }
    STAMP(5);
    {
      const unsigned off = (unsigned)(((b * 8 + 2 * w4 + (g >> 1)) * 8 + hkv) * 4096 + (lane & 31) * 16);
#pragma unroll
      for (int qt = 0; qt < 2; ++qt)
#pragma unroll
        for (int i = 0; i < 4; ++i) {
          u32x4 v;
#pragma unroll
          for (int m = 0; m < 4; ++m) v[m] = cvtpk(acc[2 * m][qt][i], acc[2 * m + 1][qt][i]);
          __builtin_amdgcn_raw_buffer_store_b128(v, xr, off + (qt * 4 + i) * 512, 0, 16);
        }
    }
    wait_vm<0>();
    STAMP(6);
    barrier_lgkm();
  }
  barrier_lgkm();
  STAMP(7);
  {
    const unsigned off = (unsigned)(((b * 8 + hkv) * 8 + (lane >> 5)) * 4096 + wave * 512 + (lane & 31) * 16);
    u32x4 d[4];
#pragma unroll
    for (int p = 0; p < 4; ++p) d[p] = __builtin_amdgcn_raw_buffer_load_b128(xr, off + p * 8192, 0, 16);
    float sum[8];
#pragma unroll
    for (int m = 0; m < 4; ++m) {
      float lo = 0.f, hi = 0.f;
#pragma unroll
      for (int p = 0; p < 4; ++p) { lo += __uint_as_float(d[p][m] << 16); hi += __uint_as_float(d[p][m] & 0xffff0000u); }
      sum[2 * m] = lo; sum[2 * m + 1] = hi;
    }
#pragma unroll
    for (int e = 0; e < 8; ++e) {
      auto rr = __builtin_amdgcn_permlane32_swap(__float_as_uint(sum[e]), __float_as_uint(sum[e]), false, false);
      sum[e] = __uint_as_float(rr[0]) + __uint_as_float(rr[1]);
    }
    if (lane < 32) {
      u32x4 v;
#pragma unroll
      for (int m = 0; m < 4; ++m) v[m] = cvtpk(sum[2 * m], sum[2 * m + 1]);
      const int q = (lane & 15) + 16 * (wave >> 2), n = 64 * hkv + 32 * ((lane >> 4) & 1) + 8 * (wave & 3);
      *(u32x4*)(out + (size_t)b * (SQ * DM) + q * DM + n) = v;
    }
  }
  if (wave == 0) {
    gu32* tokp = (gu32*)(ws + WS_TOK + (size_t)b * 128) + (lane & 7);
    gu32* outbox = (gu32*)(ws + WS_FLAG + (size_t)(b * 8 + (lane & 7)) * 128) + hkv;
    const unsigned t2 = __hip_atomic_load(tokp, RLX_AGENT);
    if (lane < 8 && t2 != tok) __hip_atomic_store(outbox, t2, RLX_AGENT);
  }
  STAMP(8);
  STAMP_FLUSH;
}

extern "C" void kernel_launch(void* const* d_in, const int* in_sizes, int n_in, void* d_out, int out_size, void* d_ws,
                              size_t ws_size, hipStream_t stream) {
  const u16* x = (const u16*)d_in[0];
  const u16* kc = (const u16*)d_in[1];
  const u16* vc = (const u16*)d_in[2];
  const u16* wq = (const u16*)d_in[3];
  const u16* wo = (const u16*)d_in[4];
  (void)hipFuncSetAttribute((const void*)fused_kernel, hipFuncAttributeMaxDynamicSharedMemorySize, LDS_TOTAL);
  fused_kernel<<<256, 512, LDS_TOTAL, stream>>>(x, kc, vc, wq, wo, (char*)d_ws, (u16*)d_out);
}
```

```cpp
#include <hip/hip_runtime.h>
#include <hip/hip_bf16.h>

typedef __attribute__((ext_vector_type(8))) short bf16x8;
typedef __attribute__((ext_vector_type(4))) short s16x4;
typedef __attribute__((ext_vector_type(4))) float f32x4;
typedef __attribute__((ext_vector_type(16))) float f32x16;
typedef __attribute__((ext_vector_type(4))) unsigned u32x4;
typedef __attribute__((ext_vector_type(2))) unsigned u32x2;
typedef unsigned short u16;

#define DI __device__ __forceinline__
#define LDS3 __attribute__((address_space(3)))

constexpr int B_ = 32, SQ = 32, DM = 512, HQ = 32, HKV = 8, SK = 512, DH = 128;
constexpr int LDS_RING = 0;
constexpr int LDS_X = 131072;
constexpr int LDS_TOTAL = 163840;
constexpr int LDS_OIMG = LDS_RING + 65536;

#define STAMP(i) do {} while (0)
#define STAMP_DECL
#define STAMP_FLUSH do {} while (0)

typedef __attribute__((ext_vector_type(2))) __bf16 bf16x2_t;
typedef __attribute__((ext_vector_type(2))) float f32x2;
DI unsigned cvtpk(float lo, float hi) {
  f32x2 v = {lo, hi};
  return __builtin_bit_cast(unsigned, __builtin_convertvector(v, bf16x2_t));
}

DI void barrier_lgkm() { asm volatile("s_waitcnt lgkmcnt(0)\n\ts_barrier" ::: "memory"); }
DI void barrier_raw() { asm volatile("s_barrier" ::: "memory"); }

DI void glds16(const void* sbase, unsigned voff, unsigned lds_dst) {
  unsigned keep;
  asm volatile("s_mov_b32 %0, m0\n\ts_mov_b32 m0, %3\n\ts_nop 0\n\tglobal_load_lds_dwordx4 %1, %2\n\ts_mov_b32 m0, %0"
               : "=&s"(keep) : "v"(voff), "s"(sbase), "s"(lds_dst) : "memory");
}
DI void glds16_nt(const void* sbase, unsigned voff, unsigned lds_dst) {
  unsigned keep;
  asm volatile("s_mov_b32 %0, m0\n\ts_mov_b32 m0, %3\n\ts_nop 0\n\tglobal_load_lds_dwordx4 %1, %2 nt\n\ts_mov_b32 m0, %0"
               : "=&s"(keep) : "v"(voff), "s"(sbase), "s"(lds_dst) : "memory");
}
DI void gload16(u32x4& dst, const void* sbase, unsigned voff) {
  asm volatile("global_load_dwordx4 %0, %1, %2" : "=v"(dst) : "v"(voff), "s"(sbase) : "memory");
}
template <int N>
DI void wait_raw(u32x4 (&r)[8]) {
  asm volatile("s_waitcnt vmcnt(%8)"
               : "+v"(r[0]), "+v"(r[1]), "+v"(r[2]), "+v"(r[3]), "+v"(r[4]), "+v"(r[5]), "+v"(r[6]), "+v"(r[7]) : "i"(N) : "memory");
}
template <int N> DI void wait_vm() { asm volatile("s_waitcnt vmcnt(%0)" :: "i"(N) : "memory"); }

template <int RS>
DI void wload(u32x4 (&raw)[8], const u16* p, unsigned voff, int ks) {
  const u16* q = p + (size_t)(32 * ks) * RS;
#pragma unroll
  for (int j = 0; j < 8; ++j) gload16(raw[j], q + (size_t)j * RS, voff);
}
template <int QOFF>
DI void bload(bf16x8 (&bf)[2], const char* bimg, int ks, int r16, int g) {
  const unsigned boff = r16 * 1024 + (((4 * ks + g) ^ r16) << 4);
  bf[0] = *(const bf16x8*)(bimg + boff);
  bf[1] = *(const bf16x8*)(bimg + boff + QOFF);
}
template <int QOFF = 16384>
DI void wstep(const u32x4 (&raw)[8], f32x4 (&acc)[8][2], const char* bimg, bf16x8 (&bf)[2], int ksn, int r16, int g) {
  const bf16x8 b0 = bf[0], b1 = bf[1];
  bload<QOFF>(bf, bimg, ksn, r16, g);
#pragma unroll
  for (int c = 0; c < 4; ++c) {
    u32x4 f0, f1;
#pragma unroll
    for (int m = 0; m < 4; ++m) {
      f0[m] = __builtin_amdgcn_perm(raw[2 * m + 1][c], raw[2 * m][c], 0x05040100u);
      f1[m] = __builtin_amdgcn_perm(raw[2 * m + 1][c], raw[2 * m][c], 0x07060302u);
    }
    const bf16x8 a0 = __builtin_bit_cast(bf16x8, f0), a1 = __builtin_bit_cast(bf16x8, f1);
    acc[2 * c][0] = __builtin_amdgcn_mfma_f32_16x16x32_bf16(a0, b0, acc[2 * c][0], 0, 0, 0);
    acc[2 * c][1] = __builtin_amdgcn_mfma_f32_16x16x32_bf16(a0, b1, acc[2 * c][1], 0, 0, 0);
    acc[2 * c + 1][0] = __builtin_amdgcn_mfma_f32_16x16x32_bf16(a1, b0, acc[2 * c + 1][0], 0, 0, 0);
    acc[2 * c + 1][1] = __builtin_amdgcn_mfma_f32_16x16x32_bf16(a1, b1, acc[2 * c + 1][1], 0, 0, 0);
  }
}
template <int RS, bool TRIG>
DI void proj_loop(u32x4 (&raw)[4][8], f32x4 (&acc)[8][2], const u16* wp, unsigned wv, const char* bimg, int rot, int r16, int g, int stag) {
  bf16x8 bf[2];
  bload<16384>(bf, bimg, rot & 15, r16, g);
#pragma unroll 1
  for (int it = 0; it < 3; ++it) {
#pragma unroll
    for (int u = 0; u < 4; ++u) {
      const int s = 4 * it + u;
      wait_raw<16>(raw[u]);
      wload<RS>(raw[(u + 3) & 3], wp, wv, (s + 3 + rot) & 15);
      __builtin_amdgcn_sched_barrier(0);
      wstep(raw[u], acc, bimg, bf, (s + 1 + rot) & 15, r16, g);
      __builtin_amdgcn_sched_barrier(0);
      if (TRIG) { if (u == stag) barrier_raw(); }
    }
  }
  wait_raw<16>(raw[0]);
  wload<RS>(raw[3], wp, wv, (15 + rot) & 15);
  __builtin_amdgcn_sched_barrier(0);
  wstep(raw[0], acc, bimg, bf, (13 + rot) & 15, r16, g);
  wait_raw<16>(raw[1]); wstep(raw[1], acc, bimg, bf, (14 + rot) & 15, r16, g);
  wait_raw<8>(raw[2]);  wstep(raw[2], acc, bimg, bf, (15 + rot) & 15, r16, g);
  wait_raw<0>(raw[3]);  wstep(raw[3], acc, bimg, bf, (15 + rot) & 15, r16, g);
}

typedef __attribute__((address_space(1))) unsigned gu32;
#define RLX_AGENT __ATOMIC_RELAXED, __HIP_MEMORY_SCOPE_AGENT
constexpr size_t WS_XBUF = 0, XBUF_BYTES = (size_t)32 * 8 * 8 * 4096;
constexpr size_t WS_FLAG = XBUF_BYTES;
constexpr size_t WS_TOK = WS_FLAG + 256 * 128;

__global__ __launch_bounds__(512, 2) void fused_kernel(const u16* x, const u16* kc, const u16* vc, const u16* wq, const u16* wo,
                                                       char* ws, u16* out
) {
  extern __shared__ __attribute__((aligned(16))) char smem[];
  const int tid = threadIdx.x, lane = tid & 63;
  const int wave = __builtin_amdgcn_readfirstlane(tid >> 6);
  const int hkv = blockIdx.x & 7, b = blockIdx.x >> 3;
  const int w4 = wave & 3;
  const bool isW = wave >= 4;
  const int r16 = lane & 15, g = lane >> 4;
  const int r32 = lane & 31, h = lane >> 5;
  STAMP_DECL
  STAMP(0);
  const char* ximg = smem + LDS_X;
  const int stag = b & 3;
  const int rot = b & 15;
  unsigned tok = 0;
  const __amdgpu_buffer_rsrc_t xr = __builtin_amdgcn_make_buffer_rsrc(ws + WS_XBUF, 0, (int)XBUF_BYTES, 0x00020000);
  const unsigned sbase = (unsigned)(size_t)smem;
  const u16* kp = kc + (size_t)(b * HKV + hkv) * SK * DH;
  const u16* vp = vc + (size_t)(b * HKV + hkv) * SK * DH;
#define K_ONE(t) do { \
    int ln_ = lane; asm volatile("" : "+v"(ln_)); \
    const int r_ = ln_ & 15, g_ = ln_ >> 4; \
    _Pragma("unroll") for (int j = 0; j < 4; ++j) { const int row = 16 * w4 + 4 * j + g_; \
      glds16_nt(kp + (t) * 64 * DH, (row * DH + 8 * (r_ ^ (row & 15))) * 2, sbase + LDS_RING + ((t) & 3) * 32768 + (16 * w4 + 4 * j) * 256); } \
  } while (0)
#define V_ONE(t) do { \
    int ln_ = lane; asm volatile("" : "+v"(ln_)); \
    const int r_ = ln_ & 15, g_ = ln_ >> 4; \
    _Pragma("unroll") for (int j = 0; j < 4; ++j) { const int row = 16 * w4 + 4 * j + g_; \
      glds16_nt(vp + (t) * 64 * DH, (row * DH + 8 * (r_ ^ (((row & 3) << 2) | ((row >> 2) & 3)))) * 2, sbase + LDS_RING + ((t) & 3) * 32768 + 16384 + (16 * w4 + 4 * j) * 256); } \
  } while (0)
#define KP(j) do { K_ONE(2 * (j)); K_ONE(2 * (j) + 1); } while (0)
#define VP(j) do { V_ONE(2 * (j)); V_ONE(2 * (j) + 1); } while (0)

  const int rot8 = b & 7;
  const int kh = isW ? 1 : 0;
#define KS1(i) ((((i) + rot8) & 7) + 8 * kh)
  {
    const u16* xp = x + (size_t)b * SQ * DM;
#pragma unroll
    for (int i = 0; i < 4; ++i) {
      const int row = 4 * wave + i;
      const int c = (lane & ~15) | ((lane & 15) ^ (row & 15));
      glds16(xp, (unsigned)(row * DM + c * 8) * 2, sbase + LDS_X + row * 1024);
    }
  }
  bf16x8 qf[8];
  {
    char* px = smem + LDS_RING + 65536 + w4 * 16384 + lane * 16;
    const u16* wp = wq + hkv * 512 + w4 * 128;
    const unsigned wv = (unsigned)(8 * g * 4096 + r16 * 8) * 2;
    u32x4 raw[4][8];
    wload<4096>(raw[0], wp, wv, KS1(0));
    f32x4 acc[8][2];
#pragma unroll
    for (int t = 0; t < 8; ++t) { acc[t][0] = f32x4{0, 0, 0, 0}; acc[t][1] = f32x4{0, 0, 0, 0}; }
    wait_vm<8>();
    barrier_lgkm();
    STAMP(1);
    wload<4096>(raw[1], wp, wv, KS1(1));
    wload<4096>(raw[2], wp, wv, KS1(2));
    bf16x8 bf[2];
    bload<16384>(bf, ximg, KS1(0), r16, g);
#pragma unroll
    for (int i = 0; i < 8; ++i) {
      if (i < 6) wait_raw<16>(raw[i & 3]); else if (i == 6) wait_raw<8>(raw[i & 3]); else wait_raw<0>(raw[i & 3]);
      if (i + 3 < 8) wload<4096>(raw[(i + 3) & 3], wp, wv, KS1(i + 3));
      if (isW) {
        if (i == 0 || i == 1) { if (i == (stag & 1)) KP(0); }
        if (i == 3 || i == 4) { if (i - 3 == (stag & 1)) VP(0); }
      }
      __builtin_amdgcn_sched_barrier(0);
      wstep(raw[i & 3], acc, ximg, bf, KS1(i < 7 ? i + 1 : i), r16, g);
      __builtin_amdgcn_sched_barrier(0);
    }
    if (isW) {
#pragma unroll
      for (int t = 0; t < 8; ++t) { *(f32x4*)(px + (2 * t) * 1024) = acc[t][0]; *(f32x4*)(px + (2 * t + 1) * 1024) = acc[t][1]; }
    }
    barrier_lgkm();
    if (wave == 4) {
      gu32* inbox = (gu32*)(ws + WS_FLAG + (size_t)(b * 8 + hkv) * 128);
      gu32* mytokp = (gu32*)(ws + WS_TOK + (size_t)b * 128) + hkv;
      const unsigned v = __hip_atomic_load(lane < 8 ? inbox + lane : mytokp, RLX_AGENT);
      unsigned t = __builtin_amdgcn_readlane(v, 8) + 1;
      while (__any(v == t)) ++t;
      if (lane == 0) __hip_atomic_store(mytokp, t, RLX_AGENT);
    }
    if (!isW) {
#pragma unroll
      for (int t = 0; t < 8; ++t) { acc[t][0] += *(const f32x4*)(px + (2 * t) * 1024); acc[t][1] += *(const f32x4*)(px + (2 * t + 1) * 1024); }
#pragma unroll
      for (int i = 0; i < 4; ++i) {
        u32x4 a, bb;
#pragma unroll
        for (int m = 0; m < 4; ++m) {
          unsigned a_ = cvtpk(acc[2 * m][0][i], acc[2 * m + 1][0][i]);
          unsigned b_ = cvtpk(acc[2 * m][1][i], acc[2 * m + 1][1][i]);
          auto r = __builtin_amdgcn_permlane16_swap(a_, b_, false, false);
          a[m] = r[0]; bb[m] = r[1];
        }
        qf[i] = __builtin_bit_cast(bf16x8, a);
        qf[4 + i] = __builtin_bit_cast(bf16x8, bb);
      }
    }
  }
  const u16* wpo = wo + (size_t)(hkv * 512) * 512 + w4 * 128;
  const unsigned wvo = (unsigned)(8 * g * 512 + r16 * 8) * 2;
  char* stg0 = smem + LDS_X + w4 * 8192 + lane * 16;
  char* stg1 = smem + LDS_RING + (w4 >> 1) * 32768 + (w4 & 1) * 8192 + lane * 16;
  const char* oimg = smem + LDS_OIMG;
  if (!isW) {
    STAMP(2);
    unsigned kaddr[8], vaddr[8];
#pragma unroll
    for (int s = 0; s < 8; ++s) kaddr[s] = r32 * 256 + 16 * ((8 * h + s) ^ (r32 & 15));
    {
      const int blk = (lane >> 4) & 1, qq = (lane & 15) >> 2, p = lane & 3;
#pragma unroll
      for (int dt = 0; dt < 4; ++dt)
#pragma unroll
        for (int e = 0; e < 2; ++e)
          vaddr[dt * 2 + e] = (8 * e + 4 * h + qq) * 256 + 16 * (4 * (dt ^ qq) + ((2 * blk + (p >> 1)) ^ (2 * e + h))) + 8 * (p & 1) + 16384;
    }
    const float csc = 0.08838834764831845f * 1.4426950408889634f;
    float m_run = -__builtin_inff(), l_run = 0.f;
    f32x16 o[4];
#pragma unroll
    for (int dt = 0; dt < 4; ++dt)
#pragma unroll
      for (int i = 0; i < 16; ++i) o[dt][i] = 0.f;

    f32x16 lacc;
#pragma unroll
    for (int i = 0; i < 16; ++i) lacc[i] = 0.f;
    bf16x8 ones;
#pragma unroll
    for (int i = 0; i < 8; ++i) ones[i] = (short)0x3F80;
#pragma unroll 1
    for (int j = 0; j < 4; ++j) {
      barrier_lgkm();
      const char* slot = smem + LDS_RING + (j & 1) * 65536;
      f32x16 sc[4];
      bf16x8 kf[2][4];
      float mxa = m_run, mxb = m_run;
#define K_LOAD(gi, buf) do { _Pragma("unroll") for (int s_ = 0; s_ < 4; ++s_) \
        kf[buf][s_] = *(const bf16x8*)(slot + kaddr[2 * ((gi) & 3) + (s_ >> 1)] + (s_ & 1) * 8192 + ((gi) >> 2) * 32768); } while (0)
      K_LOAD(0, 0);
#pragma unroll
      for (int gi = 0; gi < 8; ++gi) {
        const int pr = gi >> 2;
        __builtin_amdgcn_sched_barrier(0);
        if (gi < 7) K_LOAD(gi + 1, (gi + 1) & 1);
        if ((gi & 3) == 0) {
#pragma unroll
          for (int i = 0; i < 16; ++i) { sc[2 * pr][i] = 0.f; sc[2 * pr + 1][i] = 0.f; }
        }
#pragma unroll
        for (int s = 0; s < 4; ++s)
          sc[2 * pr + (s & 1)] = __builtin_amdgcn_mfma_f32_32x32x16_bf16(kf[gi & 1][s], qf[2 * (gi & 3) + (s >> 1)], sc[2 * pr + (s & 1)], 0, 0, 0);
        if (gi == 5 || gi == 6) {
#pragma unroll
          for (int i = 0; i < 8; ++i) { mxa = fmaxf(mxa, sc[gi - 5][i]); mxb = fmaxf(mxb, sc[gi - 5][8 + i]); }
        }
      }
      bf16x8 pf[2], vf[2][4];
#define V_LOAD(ks, buf) do { \
        const char* vb_ = slot + ((ks) >> 2) * 32768 + 4096 * ((ks) & 3); \
        _Pragma("unroll") for (int dt = 0; dt < 4; ++dt) { \
          const s16x4 lo = __builtin_amdgcn_ds_read_tr16_b64_v4i16((LDS3 s16x4*)(vb_ + vaddr[dt * 2 + 0])); \
          const s16x4 hi = __builtin_amdgcn_ds_read_tr16_b64_v4i16((LDS3 s16x4*)(vb_ + vaddr[dt * 2 + 1])); \
          vf[buf][dt] = __builtin_shufflevector(lo, hi, 0, 1, 2, 3, 4, 5, 6, 7); } } while (0)
#define P_FMA(ks) do { const int u_ = (ks) >> 1, r0_ = 8 * ((ks) & 1); \
        _Pragma("unroll") for (int m = 0; m < 8; ++m) sc[u_][r0_ + m] = fmaf(sc[u_][r0_ + m], csc, nm); } while (0)
#define P_EXP(ks, buf) do { \
        const int u_ = (ks) >> 1, r0_ = 8 * ((ks) & 1); u32x4 pw_; \
        _Pragma("unroll") for (int m = 0; m < 4; ++m) \
          pw_[m] = cvtpk(__builtin_amdgcn_exp2f(sc[u_][r0_ + 2 * m]), __builtin_amdgcn_exp2f(sc[u_][r0_ + 2 * m + 1])); \
        pf[buf] = __builtin_bit_cast(bf16x8, pw_); } while (0)
      barrier_lgkm();
      V_LOAD(0, 0);
#pragma unroll
      for (int i = 0; i < 16; ++i) { mxa = fmaxf(mxa, sc[2][i]); mxb = fmaxf(mxb, sc[3][i]); }
      float mx = fmaxf(mxa, mxb);
      {
        auto rr = __builtin_amdgcn_permlane32_swap(__float_as_uint(mx), __float_as_uint(mx), false, false);
        mx = fmaxf(__uint_as_float(rr[0]), __uint_as_float(rr[1]));
      }
      const float alpha = __builtin_amdgcn_exp2f((m_run - mx) * csc);
      const float nm = -mx * csc;
      m_run = mx;
      P_FMA(0);
      P_FMA(1);
      P_EXP(0, 0);
      lacc[0] *= alpha;
#pragma unroll
      for (int ks = 0; ks < 8; ++ks) {
        __builtin_amdgcn_sched_barrier(0);
        if (ks < 7) { V_LOAD(ks + 1, (ks + 1) & 1); }
        lacc = __builtin_amdgcn_mfma_f32_32x32x16_bf16(ones, pf[ks & 1], lacc, 0, 0, 0);
#pragma unroll
        for (int dt = 0; dt < 4; ++dt) {
          if (ks == 0) {
#pragma unroll
            for (int i = 0; i < 16; ++i) o[dt][i] *= alpha;
          }
          o[dt] = __builtin_amdgcn_mfma_f32_32x32x16_bf16(vf[ks & 1][dt], pf[ks & 1], o[dt], 0, 0, 0);
        }
        if (ks < 7) { P_EXP(ks + 1, (ks + 1) & 1); }
        if (ks < 6) { P_FMA(ks + 2); }
        if (ks > 0 && ks < 6) {
#pragma unroll
          for (int gq = 0; gq < 4; ++gq) {
            __builtin_amdgcn_sched_group_barrier(0x008, 1, 0);
            __builtin_amdgcn_sched_group_barrier(0x100, 2, 0);
            __builtin_amdgcn_sched_group_barrier(0x400, 2, 0);
            __builtin_amdgcn_sched_group_barrier(0x002, 2, 0);
          }
          __builtin_amdgcn_sched_group_barrier(0x008, 1, 0);
          __builtin_amdgcn_sched_group_barrier(0x002, 4, 0);
        }
      }
    }
    l_run = lacc[0];
    STAMP(3);
    const float inv = 1.0f / l_run;
#pragma unroll
    for (int dt = 0; dt < 4; ++dt)
#pragma unroll
      for (int a = 0; a < 4; ++a) {
        u32x2 v;
        v[0] = cvtpk(o[dt][4 * a] * inv, o[dt][4 * a + 1] * inv);
        v[1] = cvtpk(o[dt][4 * a + 2] * inv, o[dt][4 * a + 3] * inv);
        const int c = 16 * w4 + 4 * dt + a;
        *(u32x2*)(smem + LDS_OIMG + (r32 & 15) * 1024 + (r32 >> 4) * 32768 + 16 * (c ^ (r32 & 15)) + 8 * h) = v;
      }
    {
      u32x4 raw[4][8];
      __builtin_amdgcn_sched_barrier(0);
#pragma unroll
      for (int c = 0; c < 4; ++c) wload<512>(raw[c], wpo, wvo, (11 + c + rot) & 15);
      f32x4 acc[8][2];
#pragma unroll
      for (int t = 0; t < 8; ++t) { acc[t][0] = f32x4{0, 0, 0, 0}; acc[t][1] = f32x4{0, 0, 0, 0}; }
      barrier_lgkm();
      STAMP(4);
      bf16x8 bf[2];
      bload<32768>(bf, oimg, (4 + rot) & 15, r16, g);
#pragma unroll
      for (int c = 0; c < 3; ++c) {
        const char* src = c == 0 ? stg0 : stg1 + (c == 2 ? 16384 : 0);
        u32x4 tmp[8];
#pragma unroll
        for (int j = 0; j < 8; ++j) tmp[j] = *(const u32x4*)(src + j * 1024);
        wstep<32768>(tmp, acc, oimg, bf, ((c < 2 ? 5 + c : 11) + rot) & 15, r16, g);
        __builtin_amdgcn_sched_barrier(0);
      }
      wait_raw<24>(raw[0]);
      wstep<32768>(raw[0], acc, oimg, bf, (12 + rot) & 15, r16, g);
      __builtin_amdgcn_sched_barrier(0);
      wload<512>(raw[0], wpo, wvo, (15 + rot) & 15);
      wait_raw<24>(raw[1]); wstep<32768>(raw[1], acc, oimg, bf, (13 + rot) & 15, r16, g);
      wait_raw<16>(raw[2]); wstep<32768>(raw[2], acc, oimg, bf, (14 + rot) & 15, r16, g);
      wait_raw<8>(raw[3]);  wstep<32768>(raw[3], acc, oimg, bf, (15 + rot) & 15, r16, g);
      wait_raw<0>(raw[0]);  wstep<32768>(raw[0], acc, oimg, bf, (15 + rot) & 15, r16, g);
      STAMP(5);
#pragma unroll
      for (int t = 0; t < 8; ++t) { *(f32x4*)(stg0 + t * 1024) = acc[t][0]; *(f32x4*)(stg1 + t * 1024) = acc[t][1]; }
      barrier_lgkm();
    }
    if (wave == 0) {
      gu32* tokp = (gu32*)(ws + WS_TOK + (size_t)b * 128) + (lane & 7);
      gu32* inbox = (gu32*)(ws + WS_FLAG + (size_t)(b * 8 + hkv) * 128) + (lane & 7);
      gu32* outbox = (gu32*)(ws + WS_FLAG + (size_t)(b * 8 + (lane & 7)) * 128) + hkv;
      tok = __hip_atomic_load(tokp, RLX_AGENT);
      const unsigned mytok = __builtin_amdgcn_readlane(tok, hkv);
      barrier_lgkm();
      if (lane < 8) __hip_atomic_store(outbox, tok, RLX_AGENT);
      for (unsigned spin = 0; spin < (1u << 20); ++spin) {
        const unsigned f = __hip_atomic_load(inbox, RLX_AGENT);
        const unsigned t2 = __hip_atomic_load(tokp, RLX_AGENT);
        if (lane < 8 && t2 != tok) { tok = t2; __hip_atomic_store(outbox, tok, RLX_AGENT); }
        if (__all(f == mytok)) break;
        __builtin_amdgcn_s_sleep(1);
      }
      asm volatile("" ::: "memory");
    } else {
      barrier_lgkm();
    }
  } else {
    u32x4 raw[4][8];
    const unsigned lst0 = sbase + LDS_X + w4 * 8192;
    const unsigned lst1 = sbase + LDS_RING + (w4 >> 1) * 32768 + (w4 & 1) * 8192;
    const unsigned lst2 = lst1 + 16384;
#define WO_LDS(idx, dst) do { const u16* q_ = wpo + (size_t)(32 * (((idx) + rot) & 15)) * 512; \
      _Pragma("unroll") for (int j = 0; j < 8; ++j) glds16(q_ + j * 512, wvo, (dst) + j * 1024); } while (0)
    barrier_lgkm();
    KP(1);
    VP(1);
    WO_LDS(4, lst0);
    barrier_lgkm();
    KP(2);
    wload<512>(raw[0], wpo, wvo, (0 + rot) & 15);
    wait_vm<32>();
    barrier_lgkm();
    VP(2);
    wload<512>(raw[1], wpo, wvo, (1 + rot) & 15);
    wait_vm<40>();
    barrier_lgkm();
    KP(3);
    wload<512>(raw[2], wpo, wvo, (2 + rot) & 15);
    wait_vm<40>();
    barrier_lgkm();
    VP(3);
    wload<512>(raw[3], wpo, wvo, (3 + rot) & 15);
    wait_vm<40>();
    barrier_lgkm();
    WO_LDS(5, lst1);
    wait_vm<32>();
    barrier_lgkm();
    WO_LDS(6, lst2);
    wait_vm<24>();
    barrier_lgkm();
    f32x4 acc[8][2];
#pragma unroll
    for (int t = 0; t < 8; ++t) { acc[t][0] = f32x4{0, 0, 0, 0}; acc[t][1] = f32x4{0, 0, 0, 0}; }
    STAMP(3);
    wait_raw<0>(raw[0]); wait_raw<0>(raw[1]); wait_raw<0>(raw[2]); wait_raw<0>(raw[3]);
    barrier_lgkm();
    STAMP(4);
    {
      bf16x8 bf[2];
      bload<32768>(bf, oimg, rot & 15, r16, g);
#pragma unroll
      for (int c = 0; c < 4; ++c) {
        wstep<32768>(raw[c], acc, oimg, bf, ((c < 3 ? c + 1 : 7) + rot) & 15, r16, g);
        __builtin_amdgcn_sched_barrier(0);
        wload<512>(raw[c], wpo, wvo, (7 + c + rot) & 15);
      }
      wait_raw<24>(raw[0]); wstep<32768>(raw[0], acc, oimg, bf, (8 + rot) & 15, r16, g);
      wait_raw<16>(raw[1]); wstep<32768>(raw[1], acc, oimg, bf, (9 + rot) & 15, r16, g);
      wait_raw<8>(raw[2]);  wstep<32768>(raw[2], acc, oimg, bf, (10 + rot) & 15, r16, g);
      wait_raw<0>(raw[3]);  wstep<32768>(raw[3], acc, oimg, bf, (10 + rot) & 15, r16, g);
    }
    barrier_lgkm();
#pragma unroll
    for (int t = 0; t < 8; ++t) { acc[t][0] += *(const f32x4*)(stg0 + t * 1024); acc[t][1] += *(const f32x4*)(stg1 + t * 1024); }
    STAMP(5);
    {
      const unsigned off = (unsigned)(((b * 8 + 2 * w4 + (g >> 1)) * 8 + hkv) * 4096 + (lane & 31) * 16);
#pragma unroll
      for (int qt = 0; qt < 2; ++qt)
#pragma unroll
        for (int i = 0; i < 4; ++i) {
          u32x4 v;
#pragma unroll
          for (int m = 0; m < 4; ++m) v[m] = cvtpk(acc[2 * m][qt][i], acc[2 * m + 1][qt][i]);
          __builtin_amdgcn_raw_buffer_store_b128(v, xr, off + (qt * 4 + i) * 512, 0, 16);
        }
    }
    wait_vm<0>();
    STAMP(6);
    barrier_lgkm();
  }
  barrier_lgkm();
  STAMP(7);
  {
    const unsigned off = (unsigned)(((b * 8 + hkv) * 8 + (lane >> 5)) * 4096 + wave * 512 + (lane & 31) * 16);
    u32x4 d[4];
#pragma unroll
    for (int p = 0; p < 4; ++p) d[p] = __builtin_amdgcn_raw_buffer_load_b128(xr, off + p * 8192, 0, 16);
    float sum[8];
#pragma unroll
    for (int m = 0; m < 4; ++m) {
      float lo = 0.f, hi = 0.f;
#pragma unroll
      for (int p = 0; p < 4; ++p) { lo += __uint_as_float(d[p][m] << 16); hi += __uint_as_float(d[p][m] & 0xffff0000u); }
      sum[2 * m] = lo; sum[2 * m + 1] = hi;
    }
#pragma unroll
    for (int e = 0; e < 8; ++e) {
      auto rr = __builtin_amdgcn_permlane32_swap(__float_as_uint(sum[e]), __float_as_uint(sum[e]), false, false);
      sum[e] = __uint_as_float(rr[0]) + __uint_as_float(rr[1]);
    }
    if (lane < 32) {
      u32x4 v;
#pragma unroll
      for (int m = 0; m < 4; ++m) v[m] = cvtpk(sum[2 * m], sum[2 * m + 1]);
      const int q = (lane & 15) + 16 * (wave >> 2), n = 64 * hkv + 32 * ((lane >> 4) & 1) + 8 * (wave & 3);
      *(u32x4*)(out + (size_t)b * (SQ * DM) + q * DM + n) = v;
    }
  }
  if (wave == 0) {
    gu32* tokp = (gu32*)(ws + WS_TOK + (size_t)b * 128) + (lane & 7);
    gu32* outbox = (gu32*)(ws + WS_FLAG + (size_t)(b * 8 + (lane & 7)) * 128) + hkv;
    const unsigned t2 = __hip_atomic_load(tokp, RLX_AGENT);
    if (lane < 8 && t2 != tok) __hip_atomic_store(outbox, t2, RLX_AGENT);
  }
  STAMP(8);
  STAMP_FLUSH;
}

extern "C" void kernel_launch(void* const* d_in, const int* in_sizes, int n_in, void* d_out, int out_size, void* d_ws,
                              size_t ws_size, hipStream_t stream) {
  const u16* x = (const u16*)d_in[0];
  const u16* kc = (const u16*)d_in[1];
  const u16* vc = (const u16*)d_in[2];
  const u16* wq = (const u16*)d_in[3];
  const u16* wo = (const u16*)d_in[4];
  (void)hipFuncSetAttribute((const void*)fused_kernel, hipFuncAttributeMaxDynamicSharedMemorySize, LDS_TOTAL);
  fused_kernel<<<256, 512, LDS_TOTAL, stream>>>(x, kc, vc, wq, wo, (char*)d_ws, (u16*)d_out);
}
```

```cpp
#include <hip/hip_runtime.h>
#include <hip/hip_bf16.h>

typedef __attribute__((ext_vector_type(8))) short bf16x8;
typedef __attribute__((ext_vector_type(4))) short s16x4;
typedef __attribute__((ext_vector_type(4))) float f32x4;
typedef __attribute__((ext_vector_type(16))) float f32x16;
typedef __attribute__((ext_vector_type(4))) unsigned u32x4;
typedef __attribute__((ext_vector_type(2))) unsigned u32x2;
typedef unsigned short u16;

#define DI __device__ __forceinline__
#define LDS3 __attribute__((address_space(3)))

constexpr int B_ = 32, SQ = 32, DM = 512, HQ = 32, HKV = 8, SK = 512, DH = 128;
constexpr int LDS_RING = 0;
constexpr int LDS_X = 131072;
constexpr int LDS_TOTAL = 163840;
constexpr int LDS_OIMG = LDS_RING + 65536;

#define STAMP(i) do {} while (0)
#define STAMP_DECL
#define STAMP_FLUSH do {} while (0)

typedef __attribute__((ext_vector_type(2))) __bf16 bf16x2_t;
typedef __attribute__((ext_vector_type(2))) float f32x2;
DI unsigned cvtpk(float lo, float hi) {
  f32x2 v = {lo, hi};
  return __builtin_bit_cast(unsigned, __builtin_convertvector(v, bf16x2_t));
}

DI void barrier_lgkm() { asm volatile("s_waitcnt lgkmcnt(0)\n\ts_barrier" ::: "memory"); }
DI void barrier_raw() { asm volatile("s_barrier" ::: "memory"); }

DI void lds_wait4(const char* p, unsigned want) {
  const unsigned a = (unsigned)(size_t)p;
#pragma unroll 1
  for (unsigned spin = 0; spin < (1u << 22); ++spin) {
    u32x4 v;
    asm volatile("ds_read_b128 %0, %1\n\ts_waitcnt lgkmcnt(0)" : "=&v"(v) : "v"(a) : "memory");
    const unsigned mn = min(min(v[0], v[1]), min(v[2], v[3]));
    if ((unsigned)__builtin_amdgcn_readfirstlane(mn) >= want) break;
    __builtin_amdgcn_s_sleep(1);
  }
}
DI void lds_post(char* p, unsigned val) {
  asm volatile("s_waitcnt lgkmcnt(0)\n\tds_write_b32 %0, %1" :: "v"((unsigned)(size_t)p), "v"(val) : "memory");
}

DI void glds16(const void* sbase, unsigned voff, unsigned lds_dst) {
  unsigned keep;
  asm volatile("s_mov_b32 %0, m0\n\ts_mov_b32 m0, %3\n\ts_nop 0\n\tglobal_load_lds_dwordx4 %1, %2\n\ts_mov_b32 m0, %0"
               : "=&s"(keep) : "v"(voff), "s"(sbase), "s"(lds_dst) : "memory");
}
DI void glds16_nt(const void* sbase, unsigned voff, unsigned lds_dst) {
  unsigned keep;
  asm volatile("s_mov_b32 %0, m0\n\ts_mov_b32 m0, %3\n\ts_nop 0\n\tglobal_load_lds_dwordx4 %1, %2 nt\n\ts_mov_b32 m0, %0"
               : "=&s"(keep) : "v"(voff), "s"(sbase), "s"(lds_dst) : "memory");
}
DI void gload16(u32x4& dst, const void* sbase, unsigned voff) {
  asm volatile("global_load_dwordx4 %0, %1, %2" : "=v"(dst) : "v"(voff), "s"(sbase) : "memory");
}
template <int N>
DI void wait_raw(u32x4 (&r)[8]) {
  asm volatile("s_waitcnt vmcnt(%8)"
               : "+v"(r[0]), "+v"(r[1]), "+v"(r[2]), "+v"(r[3]), "+v"(r[4]), "+v"(r[5]), "+v"(r[6]), "+v"(r[7]) : "i"(N) : "memory");
}
template <int N> DI void wait_vm() { asm volatile("s_waitcnt vmcnt(%0)" :: "i"(N) : "memory"); }

template <int RS>
DI void wload(u32x4 (&raw)[8], const u16* p, unsigned voff, int ks) {
  const u16* q = p + (size_t)(32 * ks) * RS;
#pragma unroll
  for (int j = 0; j < 8; ++j) gload16(raw[j], q + (size_t)j * RS, voff);
}
template <int QOFF>
DI void bload(bf16x8 (&bf)[2], const char* bimg, int ks, int r16, int g) {
  const unsigned boff = r16 * 1024 + (((4 * ks + g) ^ r16) << 4);
  bf[0] = *(const bf16x8*)(bimg + boff);
  bf[1] = *(const bf16x8*)(bimg + boff + QOFF);
}
template <int QOFF = 16384>
DI void wstep(const u32x4 (&raw)[8], f32x4 (&acc)[8][2], const char* bimg, bf16x8 (&bf)[2], int ksn, int r16, int g) {
  const bf16x8 b0 = bf[0], b1 = bf[1];
  bload<QOFF>(bf, bimg, ksn, r16, g);
#pragma unroll
  for (int c = 0; c < 4; ++c) {
    u32x4 f0, f1;
#pragma unroll
    for (int m = 0; m < 4; ++m) {
      f0[m] = __builtin_amdgcn_perm(raw[2 * m + 1][c], raw[2 * m][c], 0x05040100u);
      f1[m] = __builtin_amdgcn_perm(raw[2 * m + 1][c], raw[2 * m][c], 0x07060302u);
    }
    const bf16x8 a0 = __builtin_bit_cast(bf16x8, f0), a1 = __builtin_bit_cast(bf16x8, f1);
    acc[2 * c][0] = __builtin_amdgcn_mfma_f32_16x16x32_bf16(a0, b0, acc[2 * c][0], 0, 0, 0);
    acc[2 * c][1] = __builtin_amdgcn_mfma_f32_16x16x32_bf16(a0, b1, acc[2 * c][1], 0, 0, 0);
    acc[2 * c + 1][0] = __builtin_amdgcn_mfma_f32_16x16x32_bf16(a1, b0, acc[2 * c + 1][0], 0, 0, 0);
    acc[2 * c + 1][1] = __builtin_amdgcn_mfma_f32_16x16x32_bf16(a1, b1, acc[2 * c + 1][1], 0, 0, 0);
  }
}
template <int RS, bool TRIG>
DI void proj_loop(u32x4 (&raw)[4][8], f32x4 (&acc)[8][2], const u16* wp, unsigned wv, const char* bimg, int rot, int r16, int g, int stag) {
  bf16x8 bf[2];
  bload<16384>(bf, bimg, rot & 15, r16, g);
#pragma unroll 1
  for (int it = 0; it < 3; ++it) {
#pragma unroll
    for (int u = 0; u < 4; ++u) {
      const int s = 4 * it + u;
      wait_raw<16>(raw[u]);
      wload<RS>(raw[(u + 3) & 3], wp, wv, (s + 3 + rot) & 15);
      __builtin_amdgcn_sched_barrier(0);
      wstep(raw[u], acc, bimg, bf, (s + 1 + rot) & 15, r16, g);
      __builtin_amdgcn_sched_barrier(0);
      if (TRIG) { if (u == stag) barrier_raw(); }
    }
  }
  wait_raw<16>(raw[0]);
  wload<RS>(raw[3], wp, wv, (15 + rot) & 15);
  __builtin_amdgcn_sched_barrier(0);
  wstep(raw[0], acc, bimg, bf, (13 + rot) & 15, r16, g);
  wait_raw<16>(raw[1]); wstep(raw[1], acc, bimg, bf, (14 + rot) & 15, r16, g);
  wait_raw<8>(raw[2]);  wstep(raw[2], acc, bimg, bf, (15 + rot) & 15, r16, g);
  wait_raw<0>(raw[3]);  wstep(raw[3], acc, bimg, bf, (15 + rot) & 15, r16, g);
}

typedef __attribute__((address_space(1))) unsigned gu32;
#define RLX_AGENT __ATOMIC_RELAXED, __HIP_MEMORY_SCOPE_AGENT
constexpr size_t WS_XBUF = 0, XBUF_BYTES = (size_t)32 * 8 * 8 * 4096;
constexpr size_t WS_FLAG = XBUF_BYTES;
constexpr size_t WS_TOK = WS_FLAG + 256 * 128;

__global__ __launch_bounds__(512, 2) void fused_kernel(const u16* x, const u16* kc, const u16* vc, const u16* wq, const u16* wo,
                                                       char* ws, u16* out
) {
  extern __shared__ __attribute__((aligned(16))) char smem[];
  const int tid = threadIdx.x, lane = tid & 63;
  const int wave = __builtin_amdgcn_readfirstlane(tid >> 6);
  const int hkv = blockIdx.x & 7, b = blockIdx.x >> 3;
  const int w4 = wave & 3;
  const bool isW = wave >= 4;
  const int r16 = lane & 15, g = lane >> 4;
  const int r32 = lane & 31, h = lane >> 5;
  STAMP_DECL
  STAMP(0);
  const char* ximg = smem + LDS_X;
  const int stag = b & 3;
  const int rot = b & 15;
  unsigned tok = 0;
  const __amdgpu_buffer_rsrc_t xr = __builtin_amdgcn_make_buffer_rsrc(ws + WS_XBUF, 0, (int)XBUF_BYTES, 0x00020000);
  const unsigned sbase = (unsigned)(size_t)smem;
  const u16* kp = kc + (size_t)(b * HKV + hkv) * SK * DH;
  const u16* vp = vc + (size_t)(b * HKV + hkv) * SK * DH;
#define K_ONE(t) do { \
    int ln_ = lane; asm volatile("" : "+v"(ln_)); \
    const int r_ = ln_ & 15, g_ = ln_ >> 4; \
    _Pragma("unroll") for (int j = 0; j < 4; ++j) { const int row = 16 * w4 + 4 * j + g_; \
      glds16_nt(kp + (t) * 64 * DH, (row * DH + 8 * (r_ ^ (row & 15))) * 2, sbase + LDS_RING + ((t) & 3) * 32768 + (16 * w4 + 4 * j) * 256); } \
  } while (0)
#define V_ONE(t) do { \
    int ln_ = lane; asm volatile("" : "+v"(ln_)); \
    const int r_ = ln_ & 15, g_ = ln_ >> 4; \
    _Pragma("unroll") for (int j = 0; j < 4; ++j) { const int row = 16 * w4 + 4 * j + g_; \
      glds16_nt(vp + (t) * 64 * DH, (row * DH + 8 * (r_ ^ (((row & 3) << 2) | ((row >> 2) & 3)))) * 2, sbase + LDS_RING + ((t) & 3) * 32768 + 16384 + (16 * w4 + 4 * j) * 256); } \
  } while (0)
#define KP(j) do { K_ONE(2 * (j)); K_ONE(2 * (j) + 1); } while (0)
#define VP(j) do { V_ONE(2 * (j)); V_ONE(2 * (j) + 1); } while (0)

  const int rot8 = b & 7;
  const int kh = isW ? 1 : 0;
#define KS1(i) ((((i) + rot8) & 7) + 8 * kh)
  {
    const u16* xp = x + (size_t)b * SQ * DM;
#pragma unroll
    for (int i = 0; i < 4; ++i) {
      const int row = 4 * wave + i;
      const int c = (lane & ~15) | ((lane & 15) ^ (row & 15));
      glds16(xp, (unsigned)(row * DM + c * 8) * 2, sbase + LDS_X + row * 1024);
    }
  }
  bf16x8 qf[8];
  {
    char* px = smem + LDS_RING + 65536 + w4 * 16384 + lane * 16;
    const u16* wp = wq + hkv * 512 + w4 * 128;
    const unsigned wv = (unsigned)(8 * g * 4096 + r16 * 8) * 2;
    u32x4 raw[4][8];
    wload<4096>(raw[0], wp, wv, KS1(0));
    f32x4 acc[8][2];
#pragma unroll
    for (int t = 0; t < 8; ++t) { acc[t][0] = f32x4{0, 0, 0, 0}; acc[t][1] = f32x4{0, 0, 0, 0}; }
    wait_vm<8>();
    barrier_lgkm();
    STAMP(1);
    wload<4096>(raw[1], wp, wv, KS1(1));
    wload<4096>(raw[2], wp, wv, KS1(2));
    bf16x8 bf[2];
    bload<16384>(bf, ximg, KS1(0), r16, g);
#pragma unroll
    for (int i = 0; i < 8; ++i) {
      if (i < 6) wait_raw<16>(raw[i & 3]); else if (i == 6) wait_raw<8>(raw[i & 3]); else wait_raw<0>(raw[i & 3]);
      if (i + 3 < 8) wload<4096>(raw[(i + 3) & 3], wp, wv, KS1(i + 3));
      if (isW) {
        if (i < 4) { if (i == stag) KP(0); }
        if (i >= 3 && i < 7) { if (i - 3 == stag) VP(0); }
      }
      __builtin_amdgcn_sched_barrier(0);
      wstep(raw[i & 3], acc, ximg, bf, KS1(i < 7 ? i + 1 : i), r16, g);
      __builtin_amdgcn_sched_barrier(0);
    }
    if (isW) {
#pragma unroll
      for (int t = 0; t < 8; ++t) { *(f32x4*)(px + (2 * t) * 1024) = acc[t][0]; *(f32x4*)(px + (2 * t + 1) * 1024) = acc[t][1]; }
    }
    barrier_lgkm();
    if (wave == 4) {
      gu32* inbox = (gu32*)(ws + WS_FLAG + (size_t)(b * 8 + hkv) * 128);
      gu32* mytokp = (gu32*)(ws + WS_TOK + (size_t)b * 128) + hkv;
      const unsigned v = __hip_atomic_load(lane < 8 ? inbox + lane : mytokp, RLX_AGENT);
      unsigned t = __builtin_amdgcn_readlane(v, 8) + 1;
      while (__any(v == t)) ++t;
      if (lane == 0) __hip_atomic_store(mytokp, t, RLX_AGENT);
    }
    if (wave == 5) lds_post(smem + LDS_X + (lane & 7) * 4, 0u);
    if (!isW) {
#pragma unroll
      for (int t = 0; t < 8; ++t) { acc[t][0] += *(const f32x4*)(px + (2 * t) * 1024); acc[t][1] += *(const f32x4*)(px + (2 * t + 1) * 1024); }
#pragma unroll
      for (int i = 0; i < 4; ++i) {
        u32x4 a, bb;
#pragma unroll
        for (int m = 0; m < 4; ++m) {
          unsigned a_ = cvtpk(acc[2 * m][0][i], acc[2 * m + 1][0][i]);
          unsigned b_ = cvtpk(acc[2 * m][1][i], acc[2 * m + 1][1][i]);
          auto r = __builtin_amdgcn_permlane16_swap(a_, b_, false, false);
          a[m] = r[0]; bb[m] = r[1];
        }
        qf[i] = __builtin_bit_cast(bf16x8, a);
        qf[4 + i] = __builtin_bit_cast(bf16x8, bb);
      }
    }
  }
  const u16* wpo = wo + (size_t)(hkv * 512) * 512 + w4 * 128;
  const unsigned wvo = (unsigned)(8 * g * 512 + r16 * 8) * 2;
  char* stg0 = smem + LDS_X + w4 * 8192 + lane * 16;
  char* stg1 = smem + LDS_RING + (w4 >> 1) * 32768 + (w4 & 1) * 8192 + lane * 16;
  const char* oimg = smem + LDS_OIMG;
  if (!isW) {
    STAMP(2);
    unsigned kaddr[8], vaddr[8];
#pragma unroll
    for (int s = 0; s < 8; ++s) kaddr[s] = r32 * 256 + 16 * ((8 * h + s) ^ (r32 & 15));
    {
      const int blk = (lane >> 4) & 1, qq = (lane & 15) >> 2, p = lane & 3;
#pragma unroll
      for (int dt = 0; dt < 4; ++dt)
#pragma unroll
        for (int e = 0; e < 2; ++e)
          vaddr[dt * 2 + e] = (8 * e + 4 * h + qq) * 256 + 16 * (4 * (dt ^ qq) + ((2 * blk + (p >> 1)) ^ (2 * e + h))) + 8 * (p & 1) + 16384;
    }
    const float csc = 0.08838834764831845f * 1.4426950408889634f;
    float m_run = -__builtin_inff(), l_run = 0.f;
    f32x16 o[4];
#pragma unroll
    for (int dt = 0; dt < 4; ++dt)
#pragma unroll
      for (int i = 0; i < 16; ++i) o[dt][i] = 0.f;

    f32x16 lacc;
#pragma unroll
    for (int i = 0; i < 16; ++i) lacc[i] = 0.f;
    bf16x8 ones;
#pragma unroll
    for (int i = 0; i < 8; ++i) ones[i] = (short)0x3F80;
#pragma unroll 1
    for (int j = 0; j < 4; ++j) {
      if (j == 0) barrier_lgkm(); else lds_wait4(smem + LDS_X + 16, 2 * j - 1);
      const char* slot = smem + LDS_RING + (j & 1) * 65536;
      f32x16 sc[4];
      bf16x8 kf[2][4];
      float mxa = m_run, mxb = m_run;
#define K_LOAD(gi, buf) do { _Pragma("unroll") for (int s_ = 0; s_ < 4; ++s_) \
        kf[buf][s_] = *(const bf16x8*)(slot + kaddr[2 * ((gi) & 3) + (s_ >> 1)] + (s_ & 1) * 8192 + ((gi) >> 2) * 32768); } while (0)
      K_LOAD(0, 0);
#pragma unroll
      for (int gi = 0; gi < 8; ++gi) {
        const int pr = gi >> 2;
        __builtin_amdgcn_sched_barrier(0);
        if (gi < 7) K_LOAD(gi + 1, (gi + 1) & 1);
        if ((gi & 3) == 0) {
#pragma unroll
          for (int i = 0; i < 16; ++i) { sc[2 * pr][i] = 0.f; sc[2 * pr + 1][i] = 0.f; }
        }
#pragma unroll
        for (int s = 0; s < 4; ++s)
          sc[2 * pr + (s & 1)] = __builtin_amdgcn_mfma_f32_32x32x16_bf16(kf[gi & 1][s], qf[2 * (gi & 3) + (s >> 1)], sc[2 * pr + (s & 1)], 0, 0, 0);
        if (gi == 5 || gi == 6) {
#pragma unroll
          for (int i = 0; i < 8; ++i) { mxa = fmaxf(mxa, sc[gi - 5][i]); mxb = fmaxf(mxb, sc[gi - 5][8 + i]); }
        }
      }
      bf16x8 pf[2], vf[2][4];
#define V_LOAD(ks, buf) do { \
        const char* vb_ = slot + ((ks) >> 2) * 32768 + 4096 * ((ks) & 3); \
        _Pragma("unroll") for (int dt = 0; dt < 4; ++dt) { \
          const s16x4 lo = __builtin_amdgcn_ds_read_tr16_b64_v4i16((LDS3 s16x4*)(vb_ + vaddr[dt * 2 + 0])); \
          const s16x4 hi = __builtin_amdgcn_ds_read_tr16_b64_v4i16((LDS3 s16x4*)(vb_ + vaddr[dt * 2 + 1])); \
          vf[buf][dt] = __builtin_shufflevector(lo, hi, 0, 1, 2, 3, 4, 5, 6, 7); } } while (0)
#define P_FMA(ks) do { const int u_ = (ks) >> 1, r0_ = 8 * ((ks) & 1); \
        _Pragma("unroll") for (int m = 0; m < 8; ++m) sc[u_][r0_ + m] = fmaf(sc[u_][r0_ + m], csc, nm); } while (0)
#define P_EXP(ks, buf) do { \
        const int u_ = (ks) >> 1, r0_ = 8 * ((ks) & 1); u32x4 pw_; \
        _Pragma("unroll") for (int m = 0; m < 4; ++m) \
          pw_[m] = cvtpk(__builtin_amdgcn_exp2f(sc[u_][r0_ + 2 * m]), __builtin_amdgcn_exp2f(sc[u_][r0_ + 2 * m + 1])); \
        pf[buf] = __builtin_bit_cast(bf16x8, pw_); } while (0)
      lds_post(smem + LDS_X + 4 * w4, 2 * j + 1);
      if (j > 0) lds_wait4(smem + LDS_X + 16, 2 * j);
      V_LOAD(0, 0);
#pragma unroll
      for (int i = 0; i < 16; ++i) { mxa = fmaxf(mxa, sc[2][i]); mxb = fmaxf(mxb, sc[3][i]); }
      float mx = fmaxf(mxa, mxb);
      {
        auto rr = __builtin_amdgcn_permlane32_swap(__float_as_uint(mx), __float_as_uint(mx), false, false);
        mx = fmaxf(__uint_as_float(rr[0]), __uint_as_float(rr[1]));
      }
      const float alpha = __builtin_amdgcn_exp2f((m_run - mx) * csc);
      const float nm = -mx * csc;
      m_run = mx;
      P_FMA(0);
      P_FMA(1);
      P_EXP(0, 0);
      lacc[0] *= alpha;
#pragma unroll
      for (int ks = 0; ks < 8; ++ks) {
        __builtin_amdgcn_sched_barrier(0);
        if (ks < 7) { V_LOAD(ks + 1, (ks + 1) & 1); }
        lacc = __builtin_amdgcn_mfma_f32_32x32x16_bf16(ones, pf[ks & 1], lacc, 0, 0, 0);
#pragma unroll
        for (int dt = 0; dt < 4; ++dt) {
          if (ks == 0) {
#pragma unroll
            for (int i = 0; i < 16; ++i) o[dt][i] *= alpha;
          }
          o[dt] = __builtin_amdgcn_mfma_f32_32x32x16_bf16(vf[ks & 1][dt], pf[ks & 1], o[dt], 0, 0, 0);
        }
        if (ks < 7) { P_EXP(ks + 1, (ks + 1) & 1); }
        if (ks < 6) { P_FMA(ks + 2); }
        if (ks > 0 && ks < 6) {
#pragma unroll
          for (int gq = 0; gq < 4; ++gq) {
            __builtin_amdgcn_sched_group_barrier(0x008, 1, 0);
            __builtin_amdgcn_sched_group_barrier(0x100, 2, 0);
            __builtin_amdgcn_sched_group_barrier(0x400, 2, 0);
            __builtin_amdgcn_sched_group_barrier(0x002, 2, 0);
          }
          __builtin_amdgcn_sched_group_barrier(0x008, 1, 0);
          __builtin_amdgcn_sched_group_barrier(0x002, 4, 0);
        }
      }
      __builtin_amdgcn_sched_barrier(0);
      lds_post(smem + LDS_X + 4 * w4, 2 * j + 2);
    }
    l_run = lacc[0];
    lds_wait4(smem + LDS_X, 7);
    STAMP(3);
    const float inv = 1.0f / l_run;
#pragma unroll
    for (int dt = 0; dt < 4; ++dt)
#pragma unroll
      for (int a = 0; a < 4; ++a) {
        u32x2 v;
        v[0] = cvtpk(o[dt][4 * a] * inv, o[dt][4 * a + 1] * inv);
        v[1] = cvtpk(o[dt][4 * a + 2] * inv, o[dt][4 * a + 3] * inv);
        const int c = 16 * w4 + 4 * dt + a;
        *(u32x2*)(smem + LDS_OIMG + (r32 & 15) * 1024 + (r32 >> 4) * 32768 + 16 * (c ^ (r32 & 15)) + 8 * h) = v;
      }
    {
      barrier_lgkm();
      STAMP(4);
      u32x4 raw[4][8];
#pragma unroll
      for (int c = 0; c < 4; ++c) wload<512>(raw[c], wpo, wvo, (10 + c + rot) & 15);
      f32x4 acc[8][2];
#pragma unroll
      for (int t = 0; t < 8; ++t) { acc[t][0] = f32x4{0, 0, 0, 0}; acc[t][1] = f32x4{0, 0, 0, 0}; }
      bf16x8 bf[2];
      bload<32768>(bf, oimg, (4 + rot) & 15, r16, g);
#pragma unroll
      for (int c = 0; c < 2; ++c) {
        const char* src = stg1 + c * 16384;
        u32x4 tmp[8];
#pragma unroll
        for (int j = 0; j < 8; ++j) tmp[j] = *(const u32x4*)(src + j * 1024);
        wstep<32768>(tmp, acc, oimg, bf, ((c == 0 ? 5 : 10) + rot) & 15, r16, g);
        __builtin_amdgcn_sched_barrier(0);
      }
      wait_raw<24>(raw[0]);
      wstep<32768>(raw[0], acc, oimg, bf, (11 + rot) & 15, r16, g);
      __builtin_amdgcn_sched_barrier(0);
      wload<512>(raw[0], wpo, wvo, (14 + rot) & 15);
      wait_raw<24>(raw[1]);
      wstep<32768>(raw[1], acc, oimg, bf, (12 + rot) & 15, r16, g);
      __builtin_amdgcn_sched_barrier(0);
      wload<512>(raw[1], wpo, wvo, (15 + rot) & 15);
      wait_raw<24>(raw[2]); wstep<32768>(raw[2], acc, oimg, bf, (13 + rot) & 15, r16, g);
      wait_raw<16>(raw[3]); wstep<32768>(raw[3], acc, oimg, bf, (14 + rot) & 15, r16, g);
      wait_raw<8>(raw[0]);  wstep<32768>(raw[0], acc, oimg, bf, (15 + rot) & 15, r16, g);
      wait_raw<0>(raw[1]);  wstep<32768>(raw[1], acc, oimg, bf, (15 + rot) & 15, r16, g);
      STAMP(5);
#pragma unroll
      for (int t = 0; t < 8; ++t) { *(f32x4*)(stg1 + t * 1024) = acc[t][0]; *(f32x4*)(stg1 + 16384 + t * 1024) = acc[t][1]; }
      barrier_lgkm();
    }
    if (wave == 0) {
      gu32* tokp = (gu32*)(ws + WS_TOK + (size_t)b * 128) + (lane & 7);
      gu32* inbox = (gu32*)(ws + WS_FLAG + (size_t)(b * 8 + hkv) * 128) + (lane & 7);
      gu32* outbox = (gu32*)(ws + WS_FLAG + (size_t)(b * 8 + (lane & 7)) * 128) + hkv;
      tok = __hip_atomic_load(tokp, RLX_AGENT);
      const unsigned mytok = __builtin_amdgcn_readlane(tok, hkv);
      barrier_lgkm();
      if (lane < 8) __hip_atomic_store(outbox, tok, RLX_AGENT);
      for (unsigned spin = 0; spin < (1u << 20); ++spin) {
        const unsigned f = __hip_atomic_load(inbox, RLX_AGENT);
        const unsigned t2 = __hip_atomic_load(tokp, RLX_AGENT);
        if (lane < 8 && t2 != tok) { tok = t2; __hip_atomic_store(outbox, tok, RLX_AGENT); }
        if (__all(f == mytok)) break;
        __builtin_amdgcn_s_sleep(1);
      }
      asm volatile("" ::: "memory");
    } else {
      barrier_lgkm();
    }
  } else {
    u32x4 raw[4][8];
    const unsigned lst0 = sbase + LDS_X + w4 * 8192;
    const unsigned lst1 = sbase + LDS_RING + (w4 >> 1) * 32768 + (w4 & 1) * 8192;
    const unsigned lst2 = lst1 + 16384;
#define DLY 9
#define STAGGER() do { for (int d_ = 0; d_ < stag; ++d_) __builtin_amdgcn_s_sleep(DLY); } while (0)
#define WO_LDS(idx, dst) do { const u16* q_ = wpo + (size_t)(32 * (((idx) + rot) & 15)) * 512; \
      _Pragma("unroll") for (int j = 0; j < 8; ++j) glds16(q_ + j * 512, wvo, (dst) + j * 1024); } while (0)
    const char* prog = smem + LDS_X;
    char* land = smem + LDS_X + 16 + 4 * w4;
    barrier_lgkm();
    KP(1);
    VP(1);
    wait_vm<8>();  lds_post(land, 1);
    lds_wait4(prog, 1);
    KP(2);
    wload<512>(raw[0], wpo, wvo, (0 + rot) & 15);
    wait_vm<16>(); lds_post(land, 2);
    lds_wait4(prog, 2);
    VP(2);
    wload<512>(raw[1], wpo, wvo, (1 + rot) & 15);
    wait_vm<24>(); lds_post(land, 3);
    lds_wait4(prog, 3);
    KP(3);
    wload<512>(raw[2], wpo, wvo, (2 + rot) & 15);
    wait_vm<24>(); lds_post(land, 4);
    lds_wait4(prog, 4);
    VP(3);
    wload<512>(raw[3], wpo, wvo, (3 + rot) & 15);
    wait_vm<24>(); lds_post(land, 5);
    lds_wait4(prog, 5);
    WO_LDS(4, lst1);
    wait_vm<16>(); lds_post(land, 6);
    lds_wait4(prog, 6);
    WO_LDS(5, lst2);
    f32x4 acc[8][2];
#pragma unroll
    for (int t = 0; t < 8; ++t) { acc[t][0] = f32x4{0, 0, 0, 0}; acc[t][1] = f32x4{0, 0, 0, 0}; }
    STAMP(3);
    wait_raw<0>(raw[0]); wait_raw<0>(raw[1]); wait_raw<0>(raw[2]); wait_raw<0>(raw[3]);
    barrier_lgkm();
    STAMP(4);
    {
      bf16x8 bf[2];
      bload<32768>(bf, oimg, rot & 15, r16, g);
#pragma unroll
      for (int c = 0; c < 4; ++c) {
        wstep<32768>(raw[c], acc, oimg, bf, ((c < 3 ? c + 1 : 6) + rot) & 15, r16, g);
        __builtin_amdgcn_sched_barrier(0);
        wload<512>(raw[c], wpo, wvo, (6 + c + rot) & 15);
      }
      wait_raw<24>(raw[0]); wstep<32768>(raw[0], acc, oimg, bf, (7 + rot) & 15, r16, g);
      wait_raw<16>(raw[1]); wstep<32768>(raw[1], acc, oimg, bf, (8 + rot) & 15, r16, g);
      wait_raw<8>(raw[2]);  wstep<32768>(raw[2], acc, oimg, bf, (9 + rot) & 15, r16, g);
      wait_raw<0>(raw[3]);  wstep<32768>(raw[3], acc, oimg, bf, (9 + rot) & 15, r16, g);
    }
    barrier_lgkm();
#pragma unroll
    for (int t = 0; t < 8; ++t) { acc[t][0] += *(const f32x4*)(stg1 + t * 1024); acc[t][1] += *(const f32x4*)(stg1 + 16384 + t * 1024); }
    STAMP(5);
    {
      const unsigned off = (unsigned)(((b * 8 + 2 * w4 + (g >> 1)) * 8 + hkv) * 4096 + (lane & 31) * 16);
#pragma unroll
      for (int qt = 0; qt < 2; ++qt)
#pragma unroll
        for (int i = 0; i < 4; ++i) {
          u32x4 v;
#pragma unroll
          for (int m = 0; m < 4; ++m) v[m] = cvtpk(acc[2 * m][qt][i], acc[2 * m + 1][qt][i]);
          __builtin_amdgcn_raw_buffer_store_b128(v, xr, off + (qt * 4 + i) * 512, 0, 16);
        }
    }
    wait_vm<0>();
    STAMP(6);
    barrier_lgkm();
  }
  barrier_lgkm();
  STAMP(7);
  {
    const unsigned off = (unsigned)(((b * 8 + hkv) * 8 + (lane >> 5)) * 4096 + wave * 512 + (lane & 31) * 16);
    u32x4 d[4];
#pragma unroll
    for (int p = 0; p < 4; ++p) d[p] = __builtin_amdgcn_raw_buffer_load_b128(xr, off + p * 8192, 0, 16);
    float sum[8];
#pragma unroll
    for (int m = 0; m < 4; ++m) {
      float lo = 0.f, hi = 0.f;
#pragma unroll
      for (int p = 0; p < 4; ++p) { lo += __uint_as_float(d[p][m] << 16); hi += __uint_as_float(d[p][m] & 0xffff0000u); }
      sum[2 * m] = lo; sum[2 * m + 1] = hi;
    }
#pragma unroll
    for (int e = 0; e < 8; ++e) {
      auto rr = __builtin_amdgcn_permlane32_swap(__float_as_uint(sum[e]), __float_as_uint(sum[e]), false, false);
      sum[e] = __uint_as_float(rr[0]) + __uint_as_float(rr[1]);
    }
    if (lane < 32) {
      u32x4 v;
#pragma unroll
      for (int m = 0; m < 4; ++m) v[m] = cvtpk(sum[2 * m], sum[2 * m + 1]);
      const int q = (lane & 15) + 16 * (wave >> 2), n = 64 * hkv + 32 * ((lane >> 4) & 1) + 8 * (wave & 3);
      *(u32x4*)(out + (size_t)b * (SQ * DM) + q * DM + n) = v;
    }
  }
  if (wave == 0) {
    gu32* tokp = (gu32*)(ws + WS_TOK + (size_t)b * 128) + (lane & 7);
    gu32* outbox = (gu32*)(ws + WS_FLAG + (size_t)(b * 8 + (lane & 7)) * 128) + hkv;
    const unsigned t2 = __hip_atomic_load(tokp, RLX_AGENT);
    if (lane < 8 && t2 != tok) __hip_atomic_store(outbox, t2, RLX_AGENT);
  }
  STAMP(8);
  STAMP_FLUSH;
}

extern "C" void kernel_launch(void* const* d_in, const int* in_sizes, int n_in, void* d_out, int out_size, void* d_ws,
                              size_t ws_size, hipStream_t stream) {
  const u16* x = (const u16*)d_in[0];
  const u16* kc = (const u16*)d_in[1];
  const u16* vc = (const u16*)d_in[2];
  const u16* wq = (const u16*)d_in[3];
  const u16* wo = (const u16*)d_in[4];
  (void)hipFuncSetAttribute((const void*)fused_kernel, hipFuncAttributeMaxDynamicSharedMemorySize, LDS_TOTAL);
  fused_kernel<<<256, 512, LDS_TOTAL, stream>>>(x, kc, vc, wq, wo, (char*)d_ws, (u16*)d_out);
}
```

```cpp
#include <hip/hip_runtime.h>
#include <hip/hip_bf16.h>

typedef __attribute__((ext_vector_type(8))) short bf16x8;
typedef __attribute__((ext_vector_type(4))) short s16x4;
typedef __attribute__((ext_vector_type(4))) float f32x4;
typedef __attribute__((ext_vector_type(16))) float f32x16;
typedef __attribute__((ext_vector_type(4))) unsigned u32x4;
typedef __attribute__((ext_vector_type(2))) unsigned u32x2;
typedef unsigned short u16;

#define DI __device__ __forceinline__
#define LDS3 __attribute__((address_space(3)))

constexpr int B_ = 32, SQ = 32, DM = 512, HQ = 32, HKV = 8, SK = 512, DH = 128;
constexpr int LDS_RING = 0;
constexpr int LDS_X = 131072;
constexpr int LDS_TOTAL = 163840;
constexpr int LDS_OIMG = LDS_RING + 65536;

#define STAMP(i) do {} while (0)
#define STAMP_DECL
#define STAMP_FLUSH do {} while (0)

typedef __attribute__((ext_vector_type(2))) __bf16 bf16x2_t;
typedef __attribute__((ext_vector_type(2))) float f32x2;
DI unsigned cvtpk(float lo, float hi) {
  f32x2 v = {lo, hi};
  return __builtin_bit_cast(unsigned, __builtin_convertvector(v, bf16x2_t));
}

DI void barrier_lgkm() { asm volatile("s_waitcnt lgkmcnt(0)\n\ts_barrier" ::: "memory"); }
DI void barrier_raw() { asm volatile("s_barrier" ::: "memory"); }

DI void lds_wait4(const char* p, unsigned want) {
  const unsigned a = (unsigned)(size_t)p;
#pragma unroll 1
  for (unsigned spin = 0; spin < (1u << 22); ++spin) {
    u32x4 v;
    asm volatile("ds_read_b128 %0, %1\n\ts_waitcnt lgkmcnt(0)" : "=&v"(v) : "v"(a) : "memory");
    const unsigned mn = min(min(v[0], v[1]), min(v[2], v[3]));
    if ((unsigned)__builtin_amdgcn_readfirstlane(mn) >= want) break;
    __builtin_amdgcn_s_sleep(1);
  }
}
DI void lds_post(char* p, unsigned val) {
  asm volatile("s_waitcnt lgkmcnt(0)\n\tds_write_b32 %0, %1" :: "v"((unsigned)(size_t)p), "v"(val) : "memory");
}

DI void glds16(const void* sbase, unsigned voff, unsigned lds_dst) {
  unsigned keep;
  asm volatile("s_mov_b32 %0, m0\n\ts_mov_b32 m0, %3\n\ts_nop 0\n\tglobal_load_lds_dwordx4 %1, %2\n\ts_mov_b32 m0, %0"
               : "=&s"(keep) : "v"(voff), "s"(sbase), "s"(lds_dst) : "memory");
}
DI void glds16_nt(const void* sbase, unsigned voff, unsigned lds_dst) {
  unsigned keep;
  asm volatile("s_mov_b32 %0, m0\n\ts_mov_b32 m0, %3\n\ts_nop 0\n\tglobal_load_lds_dwordx4 %1, %2 nt\n\ts_mov_b32 m0, %0"
               : "=&s"(keep) : "v"(voff), "s"(sbase), "s"(lds_dst) : "memory");
}
DI void glds16_nt_m(const void* sbase, unsigned voff, unsigned lds_dst, int mask) {
  unsigned keep;
  asm volatile("s_mov_b32 %0, m0\n\ts_mov_b32 m0, %3\n\ts_mov_b32 exec_lo, %4\n\ts_mov_b32 exec_hi, %4\n\tglobal_load_lds_dwordx4 %1, %2 nt\n\ts_mov_b64 exec, -1\n\ts_mov_b32 m0, %0"
               : "=&s"(keep) : "v"(voff), "s"(sbase), "s"(lds_dst), "s"(mask) : "memory");
}
DI void gload16(u32x4& dst, const void* sbase, unsigned voff) {
  asm volatile("global_load_dwordx4 %0, %1, %2" : "=v"(dst) : "v"(voff), "s"(sbase) : "memory");
}
template <int N>
DI void wait_raw(u32x4 (&r)[8]) {
  asm volatile("s_waitcnt vmcnt(%8)"
               : "+v"(r[0]), "+v"(r[1]), "+v"(r[2]), "+v"(r[3]), "+v"(r[4]), "+v"(r[5]), "+v"(r[6]), "+v"(r[7]) : "i"(N) : "memory");
}
template <int N> DI void wait_vm() { asm volatile("s_waitcnt vmcnt(%0)" :: "i"(N) : "memory"); }

template <int RS>
DI void wload(u32x4 (&raw)[8], const u16* p, unsigned voff, int ks) {
  const u16* q = p + (size_t)(32 * ks) * RS;
#pragma unroll
  for (int j = 0; j < 8; ++j) gload16(raw[j], q + (size_t)j * RS, voff);
}
template <int QOFF>
DI void bload(bf16x8 (&bf)[2], const char* bimg, int ks, int r16, int g) {
  const unsigned boff = r16 * 1024 + (((4 * ks + g) ^ r16) << 4);
  bf[0] = *(const bf16x8*)(bimg + boff);
  bf[1] = *(const bf16x8*)(bimg + boff + QOFF);
}
template <int QOFF = 16384>
DI void wstep(const u32x4 (&raw)[8], f32x4 (&acc)[8][2], const char* bimg, bf16x8 (&bf)[2], int ksn, int r16, int g) {
  const bf16x8 b0 = bf[0], b1 = bf[1];
  bload<QOFF>(bf, bimg, ksn, r16, g);
#pragma unroll
  for (int c = 0; c < 4; ++c) {
    u32x4 f0, f1;
#pragma unroll
    for (int m = 0; m < 4; ++m) {
      f0[m] = __builtin_amdgcn_perm(raw[2 * m + 1][c], raw[2 * m][c], 0x05040100u);
      f1[m] = __builtin_amdgcn_perm(raw[2 * m + 1][c], raw[2 * m][c], 0x07060302u);
    }
    const bf16x8 a0 = __builtin_bit_cast(bf16x8, f0), a1 = __builtin_bit_cast(bf16x8, f1);
    acc[2 * c][0] = __builtin_amdgcn_mfma_f32_16x16x32_bf16(a0, b0, acc[2 * c][0], 0, 0, 0);
    acc[2 * c][1] = __builtin_amdgcn_mfma_f32_16x16x32_bf16(a0, b1, acc[2 * c][1], 0, 0, 0);
    acc[2 * c + 1][0] = __builtin_amdgcn_mfma_f32_16x16x32_bf16(a1, b0, acc[2 * c + 1][0], 0, 0, 0);
    acc[2 * c + 1][1] = __builtin_amdgcn_mfma_f32_16x16x32_bf16(a1, b1, acc[2 * c + 1][1], 0, 0, 0);
  }
}
template <int RS, bool TRIG>
DI void proj_loop(u32x4 (&raw)[4][8], f32x4 (&acc)[8][2], const u16* wp, unsigned wv, const char* bimg, int rot, int r16, int g, int stag) {
  bf16x8 bf[2];
  bload<16384>(bf, bimg, rot & 15, r16, g);
#pragma unroll 1
  for (int it = 0; it < 3; ++it) {
#pragma unroll
    for (int u = 0; u < 4; ++u) {
      const int s = 4 * it + u;
      wait_raw<16>(raw[u]);
      wload<RS>(raw[(u + 3) & 3], wp, wv, (s + 3 + rot) & 15);
      __builtin_amdgcn_sched_barrier(0);
      wstep(raw[u], acc, bimg, bf, (s + 1 + rot) & 15, r16, g);
      __builtin_amdgcn_sched_barrier(0);
      if (TRIG) { if (u == stag) barrier_raw(); }
    }
  }
  wait_raw<16>(raw[0]);
  wload<RS>(raw[3], wp, wv, (15 + rot) & 15);
  __builtin_amdgcn_sched_barrier(0);
  wstep(raw[0], acc, bimg, bf, (13 + rot) & 15, r16, g);
  wait_raw<16>(raw[1]); wstep(raw[1], acc, bimg, bf, (14 + rot) & 15, r16, g);
  wait_raw<8>(raw[2]);  wstep(raw[2], acc, bimg, bf, (15 + rot) & 15, r16, g);
  wait_raw<0>(raw[3]);  wstep(raw[3], acc, bimg, bf, (15 + rot) & 15, r16, g);
}

typedef __attribute__((address_space(1))) unsigned gu32;
#define RLX_AGENT __ATOMIC_RELAXED, __HIP_MEMORY_SCOPE_AGENT
constexpr size_t WS_XBUF = 0, XBUF_BYTES = (size_t)32 * 8 * 8 * 4096;
constexpr size_t WS_FLAG = XBUF_BYTES;
constexpr size_t WS_TOK = WS_FLAG + 256 * 128;

__global__ __launch_bounds__(512, 2) void fused_kernel(const u16* x, const u16* kc, const u16* vc, const u16* wq, const u16* wo,
                                                       char* ws, u16* out
) {
  extern __shared__ __attribute__((aligned(16))) char smem[];
  const int tid = threadIdx.x, lane = tid & 63;
  const int wave = __builtin_amdgcn_readfirstlane(tid >> 6);
  const int hkv = blockIdx.x & 7, b = blockIdx.x >> 3;
  const int w4 = wave & 3;
  const bool isW = wave >= 4;
  const int r16 = lane & 15, g = lane >> 4;
  const int r32 = lane & 31, h = lane >> 5;
  STAMP_DECL
  STAMP(0);
  const char* ximg = smem + LDS_X;
  const int stag = b & 3;
  const int rot = b & 15;
  unsigned tok = 0;
  const __amdgpu_buffer_rsrc_t xr = __builtin_amdgcn_make_buffer_rsrc(ws + WS_XBUF, 0, (int)XBUF_BYTES, 0x00020000);
  const unsigned sbase = (unsigned)(size_t)smem;
  const u16* kp = kc + (size_t)(b * HKV + hkv) * SK * DH;
  const u16* vp = vc + (size_t)(b * HKV + hkv) * SK * DH;
#define K_ONE(t) do { \
    int ln_ = lane; asm volatile("" : "+v"(ln_)); \
    const int r_ = ln_ & 15, g_ = ln_ >> 4; \
    _Pragma("unroll") for (int j = 0; j < 4; ++j) { const int row = 16 * w4 + 4 * j + g_; \
      glds16_nt(kp + (t) * 64 * DH, (row * DH + 8 * (r_ ^ (row & 15))) * 2, sbase + LDS_RING + ((t) & 3) * 32768 + (16 * w4 + 4 * j) * 256); } \
  } while (0)
#define V_ONE(t) do { \
    int ln_ = lane; asm volatile("" : "+v"(ln_)); \
    const int r_ = ln_ & 15, g_ = ln_ >> 4; \
    _Pragma("unroll") for (int j = 0; j < 4; ++j) { const int row = 16 * w4 + 4 * j + g_; \
      glds16_nt(vp + (t) * 64 * DH, (row * DH + 8 * (r_ ^ (((row & 3) << 2) | ((row >> 2) & 3)))) * 2, sbase + LDS_RING + ((t) & 3) * 32768 + 16384 + (16 * w4 + 4 * j) * 256); } \
  } while (0)
#define KP(j) do { K_ONE(2 * (j)); K_ONE(2 * (j) + 1); } while (0)
#define VP(j) do { V_ONE(2 * (j)); V_ONE(2 * (j) + 1); } while (0)

  const int rot8 = b & 7;
  const int kh = isW ? 1 : 0;
#define KS1(i) ((((i) + rot8) & 7) + 8 * kh)
  {
    const u16* xp = x + (size_t)b * SQ * DM;
#pragma unroll
    for (int i = 0; i < 4; ++i) {
      const int row = 4 * wave + i;
      const int c = (lane & ~15) | ((lane & 15) ^ (row & 15));
      glds16(xp, (unsigned)(row * DM + c * 8) * 2, sbase + LDS_X + row * 1024);
    }
  }
  bf16x8 qf[8];
  {
    char* px = smem + LDS_RING + 65536 + w4 * 16384 + lane * 16;
    const u16* wp = wq + hkv * 512 + w4 * 128;
    const unsigned wv = (unsigned)(8 * g * 4096 + r16 * 8) * 2;
    u32x4 raw[4][8];
    wload<4096>(raw[0], wp, wv, KS1(0));
    f32x4 acc[8][2];
#pragma unroll
    for (int t = 0; t < 8; ++t) { acc[t][0] = f32x4{0, 0, 0, 0}; acc[t][1] = f32x4{0, 0, 0, 0}; }
    wait_vm<8>();
    barrier_lgkm();
    STAMP(1);
    wload<4096>(raw[1], wp, wv, KS1(1));
    wload<4096>(raw[2], wp, wv, KS1(2));
    bf16x8 bf[2];
    bload<16384>(bf, ximg, KS1(0), r16, g);
#pragma unroll
    for (int i = 0; i < 8; ++i) {
      if (i < 6) wait_raw<16>(raw[i & 3]); else if (i == 6) wait_raw<8>(raw[i & 3]); else wait_raw<0>(raw[i & 3]);
      if (i + 3 < 8) wload<4096>(raw[(i + 3) & 3], wp, wv, KS1(i + 3));
      if (isW) {
        if (i < 4) { if (i == stag) KP(0); }
        if (i >= 3 && i < 7) { if (i - 3 == stag) VP(0); }
      }
      __builtin_amdgcn_sched_barrier(0);
      wstep(raw[i & 3], acc, ximg, bf, KS1(i < 7 ? i + 1 : i), r16, g);
      __builtin_amdgcn_sched_barrier(0);
    }
    if (isW) {
#pragma unroll
      for (int t = 0; t < 8; ++t) { *(f32x4*)(px + (2 * t) * 1024) = acc[t][0]; *(f32x4*)(px + (2 * t + 1) * 1024) = acc[t][1]; }
    }
    barrier_lgkm();
    if (wave == 4) {
      gu32* inbox = (gu32*)(ws + WS_FLAG + (size_t)(b * 8 + hkv) * 128);
      gu32* mytokp = (gu32*)(ws + WS_TOK + (size_t)b * 128) + hkv;
      const unsigned v = __hip_atomic_load(lane < 8 ? inbox + lane : mytokp, RLX_AGENT);
      unsigned t = __builtin_amdgcn_readlane(v, 8) + 1;
      while (__any(v == t)) ++t;
      if (lane == 0) __hip_atomic_store(mytokp, t, RLX_AGENT);
    }
    if (wave == 5) lds_post(smem + LDS_X + (lane & 7) * 4, 0u);
    if (!isW) {
#pragma unroll
      for (int t = 0; t < 8; ++t) { acc[t][0] += *(const f32x4*)(px + (2 * t) * 1024); acc[t][1] += *(const f32x4*)(px + (2 * t + 1) * 1024); }
#pragma unroll
      for (int i = 0; i < 4; ++i) {
        u32x4 a, bb;
#pragma unroll
        for (int m = 0; m < 4; ++m) {
          unsigned a_ = cvtpk(acc[2 * m][0][i], acc[2 * m + 1][0][i]);
          unsigned b_ = cvtpk(acc[2 * m][1][i], acc[2 * m + 1][1][i]);
          auto r = __builtin_amdgcn_permlane16_swap(a_, b_, false, false);
          a[m] = r[0]; bb[m] = r[1];
        }
        qf[i] = __builtin_bit_cast(bf16x8, a);
        qf[4 + i] = __builtin_bit_cast(bf16x8, bb);
      }
    }
  }
  const u16* wpo = wo + (size_t)(hkv * 512) * 512 + w4 * 128;
  const unsigned wvo = (unsigned)(8 * g * 512 + r16 * 8) * 2;
  char* stg0 = smem + LDS_X + w4 * 8192 + lane * 16;
  char* stg1 = smem + LDS_RING + (w4 >> 1) * 32768 + (w4 & 1) * 8192 + lane * 16;
  const char* oimg = smem + LDS_OIMG;
  if (!isW) {
    STAMP(2);
    unsigned kaddr[8], vaddr[8];
#pragma unroll
    for (int s = 0; s < 8; ++s) kaddr[s] = r32 * 256 + 16 * ((8 * h + s) ^ (r32 & 15));
    {
      const int blk = (lane >> 4) & 1, qq = (lane & 15) >> 2, p = lane & 3;
#pragma unroll
      for (int dt = 0; dt < 4; ++dt)
#pragma unroll
        for (int e = 0; e < 2; ++e)
          vaddr[dt * 2 + e] = (8 * e + 4 * h + qq) * 256 + 16 * (4 * (dt ^ qq) + ((2 * blk + (p >> 1)) ^ (2 * e + h))) + 8 * (p & 1) + 16384;
    }
    const float csc = 0.08838834764831845f * 1.4426950408889634f;
    float m_run = -__builtin_inff(), l_run = 0.f;
    f32x16 o[4];
#pragma unroll
    for (int dt = 0; dt < 4; ++dt)
#pragma unroll
      for (int i = 0; i < 16; ++i) o[dt][i] = 0.f;

    f32x16 lacc;
#pragma unroll
    for (int i = 0; i < 16; ++i) lacc[i] = 0.f;
    bf16x8 ones;
#pragma unroll
    for (int i = 0; i < 8; ++i) ones[i] = (short)0x3F80;
    const unsigned drb = (16 * w4 + g) * 256, dkx = (r16 ^ g) << 4, dvx = (r16 ^ (4 * g)) << 4;
#define DMA_ITEM(code, i) do { const unsigned c_ = __builtin_amdgcn_readfirstlane(code); \
      const unsigned isv_ = (c_ >> 2) & 1u, p_ = c_ & 3u, t_ = 2 * p_ + ((i) >> 2), jj_ = (i) & 3; \
      const u16* src_ = (isv_ ? vp : kp) + t_ * (64 * DH) + jj_ * 512; \
      const unsigned vo_ = drb + ((isv_ ? dvx : dkx) ^ (isv_ ? (jj_ << 4) : (jj_ << 6))); \
      glds16_nt_m(src_, vo_, sbase + LDS_RING + (t_ & 3) * 32768 + isv_ * 16384 + (16 * w4 + 4 * jj_) * 256, __builtin_amdgcn_readfirstlane(c_ < 8u ? -1 : 0)); } while (0)
    char* prog = smem + LDS_X;
#pragma unroll 1
    for (int j = 0; j < 4; ++j) {
      const unsigned cq = j == 0 ? 1u : j == 1 ? 6u : j == 2 ? 7u : 0xFu;
      const unsigned cp0 = j == 0 ? 5u : j == 1 ? 3u : 0xFu, cp1 = j == 0 ? 2u : 0xFu;
      if (j == 0) barrier_lgkm();
      else {
        if (j < 3) wait_vm<16>(); else wait_vm<8>();
        lds_post(prog + 16 + 4 * w4, 2 * j - 1);
        lds_wait4(prog, 2 * j);
        lds_wait4(prog + 16, 2 * j - 1);
      }
      const char* slot = smem + LDS_RING + (j & 1) * 65536;
      f32x16 sc[4];
      bf16x8 kf[2][4];
      float mxa = m_run, mxb = m_run;
#define K_LOAD(gi, buf) do { _Pragma("unroll") for (int s_ = 0; s_ < 4; ++s_) \
        kf[buf][s_] = *(const bf16x8*)(slot + kaddr[2 * ((gi) & 3) + (s_ >> 1)] + (s_ & 1) * 8192 + ((gi) >> 2) * 32768); } while (0)
      K_LOAD(0, 0);
#pragma unroll
      for (int gi = 0; gi < 8; ++gi) {
        const int pr = gi >> 2;
        __builtin_amdgcn_sched_barrier(0);
        DMA_ITEM(cq, gi);
        if (gi < 7) K_LOAD(gi + 1, (gi + 1) & 1);
        if ((gi & 3) == 0) {
#pragma unroll
          for (int i = 0; i < 16; ++i) { sc[2 * pr][i] = 0.f; sc[2 * pr + 1][i] = 0.f; }
        }
#pragma unroll
        for (int s = 0; s < 4; ++s)
          sc[2 * pr + (s & 1)] = __builtin_amdgcn_mfma_f32_32x32x16_bf16(kf[gi & 1][s], qf[2 * (gi & 3) + (s >> 1)], sc[2 * pr + (s & 1)], 0, 0, 0);
        if (gi == 5 || gi == 6) {
#pragma unroll
          for (int i = 0; i < 8; ++i) { mxa = fmaxf(mxa, sc[gi - 5][i]); mxb = fmaxf(mxb, sc[gi - 5][8 + i]); }
        }
      }
      bf16x8 pf[2], vf[2][4];
#define V_LOAD(ks, buf) do { \
        const char* vb_ = slot + ((ks) >> 2) * 32768 + 4096 * ((ks) & 3); \
        _Pragma("unroll") for (int dt = 0; dt < 4; ++dt) { \
          const s16x4 lo = __builtin_amdgcn_ds_read_tr16_b64_v4i16((LDS3 s16x4*)(vb_ + vaddr[dt * 2 + 0])); \
          const s16x4 hi = __builtin_amdgcn_ds_read_tr16_b64_v4i16((LDS3 s16x4*)(vb_ + vaddr[dt * 2 + 1])); \
          vf[buf][dt] = __builtin_shufflevector(lo, hi, 0, 1, 2, 3, 4, 5, 6, 7); } } while (0)
#define P_FMA(ks) do { const int u_ = (ks) >> 1, r0_ = 8 * ((ks) & 1); \
        _Pragma("unroll") for (int m = 0; m < 8; ++m) sc[u_][r0_ + m] = fmaf(sc[u_][r0_ + m], csc, nm); } while (0)
#define P_EXP(ks, buf) do { \
        const int u_ = (ks) >> 1, r0_ = 8 * ((ks) & 1); u32x4 pw_; \
        _Pragma("unroll") for (int m = 0; m < 4; ++m) \
          pw_[m] = cvtpk(__builtin_amdgcn_exp2f(sc[u_][r0_ + 2 * m]), __builtin_amdgcn_exp2f(sc[u_][r0_ + 2 * m + 1])); \
        pf[buf] = __builtin_bit_cast(bf16x8, pw_); } while (0)
      lds_post(prog + 4 * w4, 2 * j + 1);
      if (j > 0) {
        if (j == 1) wait_vm<8>(); else if (j == 2) wait_vm<16>(); else wait_vm<0>();
        lds_post(prog + 16 + 4 * w4, 2 * j);
        lds_wait4(prog + 16, 2 * j);
      }
      if (j < 2) lds_wait4(prog, 2 * j + 1);
      V_LOAD(0, 0);
#pragma unroll
      for (int i = 0; i < 16; ++i) { mxa = fmaxf(mxa, sc[2][i]); mxb = fmaxf(mxb, sc[3][i]); }
      float mx = fmaxf(mxa, mxb);
      {
        auto rr = __builtin_amdgcn_permlane32_swap(__float_as_uint(mx), __float_as_uint(mx), false, false);
        mx = fmaxf(__uint_as_float(rr[0]), __uint_as_float(rr[1]));
      }
      const float alpha = __builtin_amdgcn_exp2f((m_run - mx) * csc);
      const float nm = -mx * csc;
      m_run = mx;
      P_FMA(0);
      P_FMA(1);
      P_EXP(0, 0);
      lacc[0] *= alpha;
#pragma unroll
      for (int ks = 0; ks < 8; ++ks) {
        __builtin_amdgcn_sched_barrier(0);
        DMA_ITEM(cp0, ks);
        DMA_ITEM(cp1, ks);
        if (ks < 7) { V_LOAD(ks + 1, (ks + 1) & 1); }
        lacc = __builtin_amdgcn_mfma_f32_32x32x16_bf16(ones, pf[ks & 1], lacc, 0, 0, 0);
#pragma unroll
        for (int dt = 0; dt < 4; ++dt) {
          if (ks == 0) {
#pragma unroll
            for (int i = 0; i < 16; ++i) o[dt][i] *= alpha;
          }
          o[dt] = __builtin_amdgcn_mfma_f32_32x32x16_bf16(vf[ks & 1][dt], pf[ks & 1], o[dt], 0, 0, 0);
        }
        if (ks < 7) { P_EXP(ks + 1, (ks + 1) & 1); }
        if (ks < 6) { P_FMA(ks + 2); }
        if (ks > 0 && ks < 6) {
#pragma unroll
          for (int gq = 0; gq < 4; ++gq) {
            __builtin_amdgcn_sched_group_barrier(0x008, 1, 0);
            __builtin_amdgcn_sched_group_barrier(0x100, 2, 0);
            __builtin_amdgcn_sched_group_barrier(0x400, 2, 0);
            __builtin_amdgcn_sched_group_barrier(0x002, 2, 0);
          }
          __builtin_amdgcn_sched_group_barrier(0x008, 1, 0);
          __builtin_amdgcn_sched_group_barrier(0x002, 4, 0);
        }
      }
      __builtin_amdgcn_sched_barrier(0);
      lds_post(prog + 4 * w4, 2 * j + 2);
    }
    l_run = lacc[0];
    lds_wait4(smem + LDS_X, 7);
    STAMP(3);
    const float inv = 1.0f / l_run;
#pragma unroll
    for (int dt = 0; dt < 4; ++dt)
#pragma unroll
      for (int a = 0; a < 4; ++a) {
        u32x2 v;
        v[0] = cvtpk(o[dt][4 * a] * inv, o[dt][4 * a + 1] * inv);
        v[1] = cvtpk(o[dt][4 * a + 2] * inv, o[dt][4 * a + 3] * inv);
        const int c = 16 * w4 + 4 * dt + a;
        *(u32x2*)(smem + LDS_OIMG + (r32 & 15) * 1024 + (r32 >> 4) * 32768 + 16 * (c ^ (r32 & 15)) + 8 * h) = v;
      }
    {
      barrier_lgkm();
      STAMP(4);
      u32x4 raw[4][8];
#pragma unroll
      for (int c = 0; c < 4; ++c) wload<512>(raw[c], wpo, wvo, (10 + c + rot) & 15);
      f32x4 acc[8][2];
#pragma unroll
      for (int t = 0; t < 8; ++t) { acc[t][0] = f32x4{0, 0, 0, 0}; acc[t][1] = f32x4{0, 0, 0, 0}; }
      bf16x8 bf[2];
      bload<32768>(bf, oimg, (4 + rot) & 15, r16, g);
#pragma unroll
      for (int c = 0; c < 2; ++c) {
        const char* src = stg1 + c * 16384;
        u32x4 tmp[8];
#pragma unroll
        for (int j = 0; j < 8; ++j) tmp[j] = *(const u32x4*)(src + j * 1024);
        wstep<32768>(tmp, acc, oimg, bf, ((c == 0 ? 5 : 10) + rot) & 15, r16, g);
        __builtin_amdgcn_sched_barrier(0);
      }
      wait_raw<24>(raw[0]);
      wstep<32768>(raw[0], acc, oimg, bf, (11 + rot) & 15, r16, g);
      __builtin_amdgcn_sched_barrier(0);
      wload<512>(raw[0], wpo, wvo, (14 + rot) & 15);
      wait_raw<24>(raw[1]);
      wstep<32768>(raw[1], acc, oimg, bf, (12 + rot) & 15, r16, g);
      __builtin_amdgcn_sched_barrier(0);
      wload<512>(raw[1], wpo, wvo, (15 + rot) & 15);
      wait_raw<24>(raw[2]); wstep<32768>(raw[2], acc, oimg, bf, (13 + rot) & 15, r16, g);
      wait_raw<16>(raw[3]); wstep<32768>(raw[3], acc, oimg, bf, (14 + rot) & 15, r16, g);
      wait_raw<8>(raw[0]);  wstep<32768>(raw[0], acc, oimg, bf, (15 + rot) & 15, r16, g);
      wait_raw<0>(raw[1]);  wstep<32768>(raw[1], acc, oimg, bf, (15 + rot) & 15, r16, g);
      STAMP(5);
#pragma unroll
      for (int t = 0; t < 8; ++t) { *(f32x4*)(stg1 + t * 1024) = acc[t][0]; *(f32x4*)(stg1 + 16384 + t * 1024) = acc[t][1]; }
      barrier_lgkm();
    }
    if (wave == 0) {
      gu32* tokp = (gu32*)(ws + WS_TOK + (size_t)b * 128) + (lane & 7);
      gu32* inbox = (gu32*)(ws + WS_FLAG + (size_t)(b * 8 + hkv) * 128) + (lane & 7);
      gu32* outbox = (gu32*)(ws + WS_FLAG + (size_t)(b * 8 + (lane & 7)) * 128) + hkv;
      tok = __hip_atomic_load(tokp, RLX_AGENT);
      const unsigned mytok = __builtin_amdgcn_readlane(tok, hkv);
      barrier_lgkm();
      if (lane < 8) __hip_atomic_store(outbox, tok, RLX_AGENT);
      for (unsigned spin = 0; spin < (1u << 20); ++spin) {
        const unsigned f = __hip_atomic_load(inbox, RLX_AGENT);
        const unsigned t2 = __hip_atomic_load(tokp, RLX_AGENT);
        if (lane < 8 && t2 != tok) { tok = t2; __hip_atomic_store(outbox, tok, RLX_AGENT); }
        if (__all(f == mytok)) break;
        __builtin_amdgcn_s_sleep(1);
      }
      asm volatile("" ::: "memory");
    } else {
      barrier_lgkm();
    }
  } else {
    u32x4 raw[4][8];
    const unsigned lst0 = sbase + LDS_X + w4 * 8192;
    const unsigned lst1 = sbase + LDS_RING + (w4 >> 1) * 32768 + (w4 & 1) * 8192;
    const unsigned lst2 = lst1 + 16384;
#define DLY 9
#define STAGGER() do { for (int d_ = 0; d_ < stag; ++d_) __builtin_amdgcn_s_sleep(DLY); } while (0)
#define WO_LDS(idx, dst) do { const u16* q_ = wpo + (size_t)(32 * (((idx) + rot) & 15)) * 512; \
      _Pragma("unroll") for (int j = 0; j < 8; ++j) glds16(q_ + j * 512, wvo, (dst) + j * 1024); } while (0)
    const char* prog = smem + LDS_X;
    barrier_lgkm();
    lds_wait4(prog, 2);
    wload<512>(raw[0], wpo, wvo, (0 + rot) & 15);
    lds_wait4(prog, 3);
    wload<512>(raw[1], wpo, wvo, (1 + rot) & 15);
    lds_wait4(prog, 4);
    wload<512>(raw[2], wpo, wvo, (2 + rot) & 15);
    lds_wait4(prog, 5);
    wload<512>(raw[3], wpo, wvo, (3 + rot) & 15);
    WO_LDS(4, lst1);
    lds_wait4(prog, 6);
    WO_LDS(5, lst2);
    f32x4 acc[8][2];
#pragma unroll
    for (int t = 0; t < 8; ++t) { acc[t][0] = f32x4{0, 0, 0, 0}; acc[t][1] = f32x4{0, 0, 0, 0}; }
    STAMP(3);
    wait_raw<0>(raw[0]); wait_raw<0>(raw[1]); wait_raw<0>(raw[2]); wait_raw<0>(raw[3]);
    barrier_lgkm();
    STAMP(4);
    {
      bf16x8 bf[2];
      bload<32768>(bf, oimg, rot & 15, r16, g);
#pragma unroll
      for (int c = 0; c < 4; ++c) {
        wstep<32768>(raw[c], acc, oimg, bf, ((c < 3 ? c + 1 : 6) + rot) & 15, r16, g);
        __builtin_amdgcn_sched_barrier(0);
        wload<512>(raw[c], wpo, wvo, (6 + c + rot) & 15);
      }
      wait_raw<24>(raw[0]); wstep<32768>(raw[0], acc, oimg, bf, (7 + rot) & 15, r16, g);
      wait_raw<16>(raw[1]); wstep<32768>(raw[1], acc, oimg, bf, (8 + rot) & 15, r16, g);
      wait_raw<8>(raw[2]);  wstep<32768>(raw[2], acc, oimg, bf, (9 + rot) & 15, r16, g);
      wait_raw<0>(raw[3]);  wstep<32768>(raw[3], acc, oimg, bf, (9 + rot) & 15, r16, g);
    }
    barrier_lgkm();
#pragma unroll
    for (int t = 0; t < 8; ++t) { acc[t][0] += *(const f32x4*)(stg1 + t * 1024); acc[t][1] += *(const f32x4*)(stg1 + 16384 + t * 1024); }
    STAMP(5);
    {
      const unsigned off = (unsigned)(((b * 8 + 2 * w4 + (g >> 1)) * 8 + hkv) * 4096 + (lane & 31) * 16);
#pragma unroll
      for (int qt = 0; qt < 2; ++qt)
#pragma unroll
        for (int i = 0; i < 4; ++i) {
          u32x4 v;
#pragma unroll
          for (int m = 0; m < 4; ++m) v[m] = cvtpk(acc[2 * m][qt][i], acc[2 * m + 1][qt][i]);
          __builtin_amdgcn_raw_buffer_store_b128(v, xr, off + (qt * 4 + i) * 512, 0, 16);
        }
    }
    wait_vm<0>();
    STAMP(6);
    barrier_lgkm();
  }
  barrier_lgkm();
  STAMP(7);
  {
    const unsigned off = (unsigned)(((b * 8 + hkv) * 8 + (lane >> 5)) * 4096 + wave * 512 + (lane & 31) * 16);
    u32x4 d[4];
#pragma unroll
    for (int p = 0; p < 4; ++p) d[p] = __builtin_amdgcn_raw_buffer_load_b128(xr, off + p * 8192, 0, 16);
    float sum[8];
#pragma unroll
    for (int m = 0; m < 4; ++m) {
      float lo = 0.f, hi = 0.f;
#pragma unroll
      for (int p = 0; p < 4; ++p) { lo += __uint_as_float(d[p][m] << 16); hi += __uint_as_float(d[p][m] & 0xffff0000u); }
      sum[2 * m] = lo; sum[2 * m + 1] = hi;
    }
#pragma unroll
    for (int e = 0; e < 8; ++e) {
      auto rr = __builtin_amdgcn_permlane32_swap(__float_as_uint(sum[e]), __float_as_uint(sum[e]), false, false);
      sum[e] = __uint_as_float(rr[0]) + __uint_as_float(rr[1]);
    }
    if (lane < 32) {
      u32x4 v;
#pragma unroll
      for (int m = 0; m < 4; ++m) v[m] = cvtpk(sum[2 * m], sum[2 * m + 1]);
      const int q = (lane & 15) + 16 * (wave >> 2), n = 64 * hkv + 32 * ((lane >> 4) & 1) + 8 * (wave & 3);
      *(u32x4*)(out + (size_t)b * (SQ * DM) + q * DM + n) = v;
    }
  }
  if (wave == 0) {
    gu32* tokp = (gu32*)(ws + WS_TOK + (size_t)b * 128) + (lane & 7);
    gu32* outbox = (gu32*)(ws + WS_FLAG + (size_t)(b * 8 + (lane & 7)) * 128) + hkv;
    const unsigned t2 = __hip_atomic_load(tokp, RLX_AGENT);
    if (lane < 8 && t2 != tok) __hip_atomic_store(outbox, t2, RLX_AGENT);
  }
  STAMP(8);
  STAMP_FLUSH;
}

extern "C" void kernel_launch(void* const* d_in, const int* in_sizes, int n_in, void* d_out, int out_size, void* d_ws,
                              size_t ws_size, hipStream_t stream) {
  const u16* x = (const u16*)d_in[0];
  const u16* kc = (const u16*)d_in[1];
  const u16* vc = (const u16*)d_in[2];
  const u16* wq = (const u16*)d_in[3];
  const u16* wo = (const u16*)d_in[4];
  (void)hipFuncSetAttribute((const void*)fused_kernel, hipFuncAttributeMaxDynamicSharedMemorySize, LDS_TOTAL);
  fused_kernel<<<256, 512, LDS_TOTAL, stream>>>(x, kc, vc, wq, wo, (char*)d_ws, (u16*)d_out);
}
```

```cpp
#include <hip/hip_runtime.h>
#include <hip/hip_bf16.h>

typedef __attribute__((ext_vector_type(8))) short bf16x8;
typedef __attribute__((ext_vector_type(4))) short s16x4;
typedef __attribute__((ext_vector_type(4))) float f32x4;
typedef __attribute__((ext_vector_type(16))) float f32x16;
typedef __attribute__((ext_vector_type(4))) unsigned u32x4;
typedef __attribute__((ext_vector_type(2))) unsigned u32x2;
typedef unsigned short u16;

#define DI __device__ __forceinline__
#define LDS3 __attribute__((address_space(3)))

constexpr int B_ = 32, SQ = 32, DM = 512, HQ = 32, HKV = 8, SK = 512, DH = 128;
constexpr int LDS_RING = 0;
constexpr int LDS_X = 131072;
constexpr int LDS_TOTAL = 163840;
constexpr int LDS_OIMG = LDS_RING + 65536;

#define STAMP(i) do {} while (0)
#define STAMP_DECL
#define STAMP_FLUSH do {} while (0)

typedef __attribute__((ext_vector_type(2))) __bf16 bf16x2_t;
typedef __attribute__((ext_vector_type(2))) float f32x2;
DI unsigned cvtpk(float lo, float hi) {
  f32x2 v = {lo, hi};
  return __builtin_bit_cast(unsigned, __builtin_convertvector(v, bf16x2_t));
}

DI void barrier_lgkm() { asm volatile("s_waitcnt lgkmcnt(0)\n\ts_barrier" ::: "memory"); }
DI void barrier_raw() { asm volatile("s_barrier" ::: "memory"); }

DI void glds16(const void* sbase, unsigned voff, unsigned lds_dst) {
  unsigned keep;
  asm volatile("s_mov_b32 %0, m0\n\ts_mov_b32 m0, %3\n\ts_nop 0\n\tglobal_load_lds_dwordx4 %1, %2\n\ts_mov_b32 m0, %0"
               : "=&s"(keep) : "v"(voff), "s"(sbase), "s"(lds_dst) : "memory");
}
DI void glds16_nt(const void* sbase, unsigned voff, unsigned lds_dst) {
  unsigned keep;
  asm volatile("s_mov_b32 %0, m0\n\ts_mov_b32 m0, %3\n\ts_nop 0\n\tglobal_load_lds_dwordx4 %1, %2 nt\n\ts_mov_b32 m0, %0"
               : "=&s"(keep) : "v"(voff), "s"(sbase), "s"(lds_dst) : "memory");
}
DI void gload16(u32x4& dst, const void* sbase, unsigned voff) {
  asm volatile("global_load_dwordx4 %0, %1, %2" : "=v"(dst) : "v"(voff), "s"(sbase) : "memory");
}
template <int N>
DI void wait_raw(u32x4 (&r)[8]) {
  asm volatile("s_waitcnt vmcnt(%8)"
               : "+v"(r[0]), "+v"(r[1]), "+v"(r[2]), "+v"(r[3]), "+v"(r[4]), "+v"(r[5]), "+v"(r[6]), "+v"(r[7]) : "i"(N) : "memory");
}
template <int N> DI void wait_vm() { asm volatile("s_waitcnt vmcnt(%0)" :: "i"(N) : "memory"); }

template <int RS>
DI void wload(u32x4 (&raw)[8], const u16* p, unsigned voff, int ks) {
  const u16* q = p + (size_t)(32 * ks) * RS;
#pragma unroll
  for (int j = 0; j < 8; ++j) gload16(raw[j], q + (size_t)j * RS, voff);
}
template <int QOFF>
DI void bload(bf16x8 (&bf)[2], const char* bimg, int ks, int r16, int g) {
  const unsigned boff = r16 * 1024 + (((4 * ks + g) ^ r16) << 4);
  bf[0] = *(const bf16x8*)(bimg + boff);
  bf[1] = *(const bf16x8*)(bimg + boff + QOFF);
}
template <int QOFF = 16384>
DI void wstep(const u32x4 (&raw)[8], f32x4 (&acc)[8][2], const char* bimg, bf16x8 (&bf)[2], int ksn, int r16, int g) {
  const bf16x8 b0 = bf[0], b1 = bf[1];
  bload<QOFF>(bf, bimg, ksn, r16, g);
#pragma unroll
  for (int c = 0; c < 4; ++c) {
    u32x4 f0, f1;
#pragma unroll
    for (int m = 0; m < 4; ++m) {
      f0[m] = __builtin_amdgcn_perm(raw[2 * m + 1][c], raw[2 * m][c], 0x05040100u);
      f1[m] = __builtin_amdgcn_perm(raw[2 * m + 1][c], raw[2 * m][c], 0x07060302u);
    }
    const bf16x8 a0 = __builtin_bit_cast(bf16x8, f0), a1 = __builtin_bit_cast(bf16x8, f1);
    acc[2 * c][0] = __builtin_amdgcn_mfma_f32_16x16x32_bf16(a0, b0, acc[2 * c][0], 0, 0, 0);
    acc[2 * c][1] = __builtin_amdgcn_mfma_f32_16x16x32_bf16(a0, b1, acc[2 * c][1], 0, 0, 0);
    acc[2 * c + 1][0] = __builtin_amdgcn_mfma_f32_16x16x32_bf16(a1, b0, acc[2 * c + 1][0], 0, 0, 0);
    acc[2 * c + 1][1] = __builtin_amdgcn_mfma_f32_16x16x32_bf16(a1, b1, acc[2 * c + 1][1], 0, 0, 0);
  }
}
template <int RS, bool TRIG>
DI void proj_loop(u32x4 (&raw)[4][8], f32x4 (&acc)[8][2], const u16* wp, unsigned wv, const char* bimg, int rot, int r16, int g, int stag) {
  bf16x8 bf[2];
  bload<16384>(bf, bimg, rot & 15, r16, g);
#pragma unroll 1
  for (int it = 0; it < 3; ++it) {
#pragma unroll
    for (int u = 0; u < 4; ++u) {
      const int s = 4 * it + u;
      wait_raw<16>(raw[u]);
      wload<RS>(raw[(u + 3) & 3], wp, wv, (s + 3 + rot) & 15);
      __builtin_amdgcn_sched_barrier(0);
      wstep(raw[u], acc, bimg, bf, (s + 1 + rot) & 15, r16, g);
      __builtin_amdgcn_sched_barrier(0);
      if (TRIG) { if (u == stag) barrier_raw(); }
    }
  }
  wait_raw<16>(raw[0]);
  wload<RS>(raw[3], wp, wv, (15 + rot) & 15);
  __builtin_amdgcn_sched_barrier(0);
  wstep(raw[0], acc, bimg, bf, (13 + rot) & 15, r16, g);
  wait_raw<16>(raw[1]); wstep(raw[1], acc, bimg, bf, (14 + rot) & 15, r16, g);
  wait_raw<8>(raw[2]);  wstep(raw[2], acc, bimg, bf, (15 + rot) & 15, r16, g);
  wait_raw<0>(raw[3]);  wstep(raw[3], acc, bimg, bf, (15 + rot) & 15, r16, g);
}

typedef __attribute__((address_space(1))) unsigned gu32;
#define RLX_AGENT __ATOMIC_RELAXED, __HIP_MEMORY_SCOPE_AGENT
constexpr size_t WS_XBUF = 0, XBUF_BYTES = (size_t)32 * 8 * 8 * 4096;
constexpr size_t WS_FLAG = XBUF_BYTES;
constexpr size_t WS_TOK = WS_FLAG + 256 * 128;

__global__ __launch_bounds__(512, 2) void fused_kernel(const u16* x, const u16* kc, const u16* vc, const u16* wq, const u16* wo,
                                                       char* ws, u16* out
) {
  extern __shared__ __attribute__((aligned(16))) char smem[];
  const int tid = threadIdx.x, lane = tid & 63;
  const int wave = __builtin_amdgcn_readfirstlane(tid >> 6);
  const int hkv = blockIdx.x & 7, b = blockIdx.x >> 3;
  const int w4 = wave & 3;
  const bool isW = wave >= 4;
  const int r16 = lane & 15, g = lane >> 4;
  const int r32 = lane & 31, h = lane >> 5;
  STAMP_DECL
  STAMP(0);
  const char* ximg = smem + LDS_X;
  const int stag = b & 3;
  const int rot = b & 15;
  unsigned tok = 0;
  const __amdgpu_buffer_rsrc_t xr = __builtin_amdgcn_make_buffer_rsrc(ws + WS_XBUF, 0, (int)XBUF_BYTES, 0x00020000);
  const unsigned sbase = (unsigned)(size_t)smem;
  const u16* kp = kc + (size_t)(b * HKV + hkv) * SK * DH;
  const u16* vp = vc + (size_t)(b * HKV + hkv) * SK * DH;
#define K_ONE(t) do { \
    int ln_ = lane; asm volatile("" : "+v"(ln_)); \
    const int r_ = ln_ & 15, g_ = ln_ >> 4; \
    _Pragma("unroll") for (int j = 0; j < 4; ++j) { const int row = 16 * w4 + 4 * j + g_; \
      glds16_nt(kp + (t) * 64 * DH, (row * DH + 8 * (r_ ^ (row & 15))) * 2, sbase + LDS_RING + ((t) & 3) * 32768 + (16 * w4 + 4 * j) * 256); } \
  } while (0)
#define V_ONE(t) do { \
    int ln_ = lane; asm volatile("" : "+v"(ln_)); \
    const int r_ = ln_ & 15, g_ = ln_ >> 4; \
    _Pragma("unroll") for (int j = 0; j < 4; ++j) { const int row = 16 * w4 + 4 * j + g_; \
      glds16_nt(vp + (t) * 64 * DH, (row * DH + 8 * (r_ ^ (((row & 3) << 2) | ((row >> 2) & 3)))) * 2, sbase + LDS_RING + ((t) & 3) * 32768 + 16384 + (16 * w4 + 4 * j) * 256); } \
  } while (0)
#define KP(j) do { K_ONE(2 * (j)); K_ONE(2 * (j) + 1); } while (0)
#define VP(j) do { V_ONE(2 * (j)); V_ONE(2 * (j) + 1); } while (0)

  if (!isW) {
    {
      const u16* xp = x + (size_t)b * SQ * DM;
#pragma unroll
      for (int i = 0; i < 8; ++i) {
        const int row = 8 * w4 + i;
        const int c = (lane & ~15) | ((lane & 15) ^ (row & 15));
        glds16(xp, (unsigned)(row * DM + c * 8) * 2, sbase + LDS_X + row * 1024);
      }
    }
    bf16x8 qf[8];
    {
      const u16* wp = wq + hkv * 512 + w4 * 128;
      const unsigned wv = (unsigned)(8 * g * 4096 + r16 * 8) * 2;
      u32x4 raw[4][8];
      wload<4096>(raw[0], wp, wv, (0 + rot) & 15);
      f32x4 acc[8][2];
#pragma unroll
      for (int t = 0; t < 8; ++t) { acc[t][0] = f32x4{0, 0, 0, 0}; acc[t][1] = f32x4{0, 0, 0, 0}; }
      wait_vm<8>();
      barrier_lgkm();
      wload<4096>(raw[1], wp, wv, (1 + rot) & 15);
      wload<4096>(raw[2], wp, wv, (2 + rot) & 15);
      STAMP(1);
      proj_loop<4096, true>(raw, acc, wp, wv, ximg, rot, r16, g, stag);
#pragma unroll
      for (int i = 0; i < 4; ++i) {
        u32x4 a, bb;
#pragma unroll
        for (int m = 0; m < 4; ++m) {
          unsigned a_ = cvtpk(acc[2 * m][0][i], acc[2 * m + 1][0][i]);
          unsigned b_ = cvtpk(acc[2 * m][1][i], acc[2 * m + 1][1][i]);
          auto r = __builtin_amdgcn_permlane16_swap(a_, b_, false, false);
          a[m] = r[0]; bb[m] = r[1];
        }
        qf[i] = __builtin_bit_cast(bf16x8, a);
        qf[4 + i] = __builtin_bit_cast(bf16x8, bb);
      }
    }
    STAMP(2);
    unsigned kaddr[8], vaddr[8];
#pragma unroll
    for (int s = 0; s < 8; ++s) kaddr[s] = r32 * 256 + 16 * ((8 * h + s) ^ (r32 & 15));
    {
      const int blk = (lane >> 4) & 1, qq = (lane & 15) >> 2, p = lane & 3;
#pragma unroll
      for (int dt = 0; dt < 4; ++dt)
#pragma unroll
        for (int e = 0; e < 2; ++e)
          vaddr[dt * 2 + e] = (8 * e + 4 * h + qq) * 256 + 16 * (4 * (dt ^ qq) + ((2 * blk + (p >> 1)) ^ (2 * e + h))) + 8 * (p & 1) + 16384;
    }
    const float csc = 0.08838834764831845f * 1.4426950408889634f;
    float m_run = -__builtin_inff(), l_run = 0.f;
    f32x16 o[4];
#pragma unroll
    for (int dt = 0; dt < 4; ++dt)
#pragma unroll
      for (int i = 0; i < 16; ++i) o[dt][i] = 0.f;

    f32x16 lacc;
#pragma unroll
    for (int i = 0; i < 16; ++i) lacc[i] = 0.f;
    bf16x8 ones;
#pragma unroll
    for (int i = 0; i < 8; ++i) ones[i] = (short)0x3F80;
#pragma unroll 1
    for (int j = 0; j < 4; ++j) {
      barrier_lgkm();
      const char* slot = smem + LDS_RING + (j & 1) * 65536;
      f32x16 sc[4];
      bf16x8 kf[2][4];
      float mxa = m_run, mxb = m_run;
#define K_LOAD(gi, buf) do { _Pragma("unroll") for (int s_ = 0; s_ < 4; ++s_) \
        kf[buf][s_] = *(const bf16x8*)(slot + kaddr[2 * ((gi) & 3) + (s_ >> 1)] + (s_ & 1) * 8192 + ((gi) >> 2) * 32768); } while (0)
      K_LOAD(0, 0);
#pragma unroll
      for (int gi = 0; gi < 8; ++gi) {
        const int pr = gi >> 2;
        __builtin_amdgcn_sched_barrier(0);
        if (gi < 7) K_LOAD(gi + 1, (gi + 1) & 1);
        if ((gi & 3) == 0) {
#pragma unroll
          for (int i = 0; i < 16; ++i) { sc[2 * pr][i] = 0.f; sc[2 * pr + 1][i] = 0.f; }
        }
#pragma unroll
        for (int s = 0; s < 4; ++s)
          sc[2 * pr + (s & 1)] = __builtin_amdgcn_mfma_f32_32x32x16_bf16(kf[gi & 1][s], qf[2 * (gi & 3) + (s >> 1)], sc[2 * pr + (s & 1)], 0, 0, 0);
        if (gi == 5 || gi == 6) {
#pragma unroll
          for (int i = 0; i < 8; ++i) { mxa = fmaxf(mxa, sc[gi - 5][i]); mxb = fmaxf(mxb, sc[gi - 5][8 + i]); }
        }
      }
      bf16x8 pf[2], vf[2][4];
#define V_LOAD(ks, buf) do { \
        const char* vb_ = slot + ((ks) >> 2) * 32768 + 4096 * ((ks) & 3); \
        _Pragma("unroll") for (int dt = 0; dt < 4; ++dt) { \
          const s16x4 lo = __builtin_amdgcn_ds_read_tr16_b64_v4i16((LDS3 s16x4*)(vb_ + vaddr[dt * 2 + 0])); \
          const s16x4 hi = __builtin_amdgcn_ds_read_tr16_b64_v4i16((LDS3 s16x4*)(vb_ + vaddr[dt * 2 + 1])); \
          vf[buf][dt] = __builtin_shufflevector(lo, hi, 0, 1, 2, 3, 4, 5, 6, 7); } } while (0)
#define P_FMA(ks) do { const int u_ = (ks) >> 1, r0_ = 8 * ((ks) & 1); \
        _Pragma("unroll") for (int m = 0; m < 8; ++m) sc[u_][r0_ + m] = fmaf(sc[u_][r0_ + m], csc, nm); } while (0)
#define P_EXP(ks, buf) do { \
        const int u_ = (ks) >> 1, r0_ = 8 * ((ks) & 1); u32x4 pw_; \
        _Pragma("unroll") for (int m = 0; m < 4; ++m) \
          pw_[m] = cvtpk(__builtin_amdgcn_exp2f(sc[u_][r0_ + 2 * m]), __builtin_amdgcn_exp2f(sc[u_][r0_ + 2 * m + 1])); \
        pf[buf] = __builtin_bit_cast(bf16x8, pw_); } while (0)
      barrier_lgkm();
      V_LOAD(0, 0);
#pragma unroll
      for (int i = 0; i < 16; ++i) { mxa = fmaxf(mxa, sc[2][i]); mxb = fmaxf(mxb, sc[3][i]); }
      float mx = fmaxf(mxa, mxb);
      {
        auto rr = __builtin_amdgcn_permlane32_swap(__float_as_uint(mx), __float_as_uint(mx), false, false);
        mx = fmaxf(__uint_as_float(rr[0]), __uint_as_float(rr[1]));
      }
      const float alpha = __builtin_amdgcn_exp2f((m_run - mx) * csc);
      const float nm = -mx * csc;
      m_run = mx;
      P_FMA(0);
      P_FMA(1);
      P_EXP(0, 0);
      lacc[0] *= alpha;
#pragma unroll
      for (int ks = 0; ks < 8; ++ks) {
        __builtin_amdgcn_sched_barrier(0);
        if (ks < 7) { V_LOAD(ks + 1, (ks + 1) & 1); }
        lacc = __builtin_amdgcn_mfma_f32_32x32x16_bf16(ones, pf[ks & 1], lacc, 0, 0, 0);
#pragma unroll
        for (int dt = 0; dt < 4; ++dt) {
          if (ks == 0) {
#pragma unroll
            for (int i = 0; i < 16; ++i) o[dt][i] *= alpha;
          }
          o[dt] = __builtin_amdgcn_mfma_f32_32x32x16_bf16(vf[ks & 1][dt], pf[ks & 1], o[dt], 0, 0, 0);
        }
        if (ks < 7) { P_EXP(ks + 1, (ks + 1) & 1); }
        if (ks < 6) { P_FMA(ks + 2); }
        if (ks > 0 && ks < 6) {
#pragma unroll
          for (int gq = 0; gq < 4; ++gq) {
            __builtin_amdgcn_sched_group_barrier(0x008, 1, 0);
            __builtin_amdgcn_sched_group_barrier(0x100, 2, 0);
            __builtin_amdgcn_sched_group_barrier(0x400, 2, 0);
            __builtin_amdgcn_sched_group_barrier(0x002, 2, 0);
          }
          __builtin_amdgcn_sched_group_barrier(0x008, 1, 0);
          __builtin_amdgcn_sched_group_barrier(0x002, 4, 0);
        }
      }
    }
    l_run = lacc[0];
    STAMP(3);
    const float inv = 1.0f / l_run;
#pragma unroll
    for (int dt = 0; dt < 4; ++dt)
#pragma unroll
      for (int a = 0; a < 4; ++a) {
        u32x2 v;
        v[0] = cvtpk(o[dt][4 * a] * inv, o[dt][4 * a + 1] * inv);
        v[1] = cvtpk(o[dt][4 * a + 2] * inv, o[dt][4 * a + 3] * inv);
        const int c = 16 * w4 + 4 * dt + a;
        *(u32x2*)(smem + LDS_OIMG + (r32 & 15) * 1024 + (r32 >> 4) * 32768 + 16 * (c ^ (r32 & 15)) + 8 * h) = v;
      }
    barrier_lgkm();
    STAMP(4);
    if (wave == 0) {
      gu32* tokp = (gu32*)(ws + WS_TOK + (size_t)b * 128) + (lane & 7);
      gu32* inbox = (gu32*)(ws + WS_FLAG + (size_t)(b * 8 + hkv) * 128) + (lane & 7);
      gu32* outbox = (gu32*)(ws + WS_FLAG + (size_t)(b * 8 + (lane & 7)) * 128) + hkv;
      tok = __hip_atomic_load(tokp, RLX_AGENT);
      const unsigned mytok = __builtin_amdgcn_readlane(tok, hkv);
      barrier_lgkm();
      if (lane < 8) __hip_atomic_store(outbox, tok, RLX_AGENT);
      for (unsigned spin = 0; spin < (1u << 20); ++spin) {
        const unsigned f = __hip_atomic_load(inbox, RLX_AGENT);
        const unsigned t2 = __hip_atomic_load(tokp, RLX_AGENT);
        if (lane < 8 && t2 != tok) { tok = t2; __hip_atomic_store(outbox, tok, RLX_AGENT); }
        if (__all(f == mytok)) break;
        __builtin_amdgcn_s_sleep(1);
      }
      asm volatile("" ::: "memory");
    } else {
      barrier_lgkm();
    }
  } else {
    barrier_lgkm();
    STAMP(1);
    if (wave == 4) {
      gu32* inbox = (gu32*)(ws + WS_FLAG + (size_t)(b * 8 + hkv) * 128);
      gu32* mytokp = (gu32*)(ws + WS_TOK + (size_t)b * 128) + hkv;
      const unsigned v = __hip_atomic_load(lane < 8 ? inbox + lane : mytokp, RLX_AGENT);
      unsigned t = __builtin_amdgcn_readlane(v, 8) + 1;
      while (__any(v == t)) ++t;
      if (lane == 0) __hip_atomic_store(mytokp, t, RLX_AGENT);
    }
    const u16* wp = wo + (size_t)(hkv * 512) * 512 + w4 * 128;
    const unsigned wv = (unsigned)(8 * g * 512 + r16 * 8) * 2;
    u32x4 raw[4][8];
    const unsigned lst0 = sbase + LDS_X + w4 * 8192;
    const unsigned lst1 = sbase + LDS_RING + (w4 >> 1) * 32768 + (w4 & 1) * 8192;
    const unsigned lst2 = lst1 + 16384;
#define WO_LDS(idx, dst) do { const u16* q_ = wp + (size_t)(32 * (((idx) + rot) & 15)) * 512; \
      _Pragma("unroll") for (int j = 0; j < 8; ++j) glds16(q_ + j * 512, wv, (dst) + j * 1024); } while (0)
    barrier_raw();
    KP(0);
    barrier_raw();
    VP(0);
    barrier_raw();
    KP(1);
    wait_vm<16>();
    barrier_lgkm();
    VP(1);
    WO_LDS(4, lst0);
    wait_vm<24>();
    barrier_lgkm();
    KP(2);
    wload<512>(raw[0], wp, wv, (0 + rot) & 15);
    wait_vm<32>();
    barrier_lgkm();
    VP(2);
    wload<512>(raw[1], wp, wv, (1 + rot) & 15);
    wait_vm<40>();
    barrier_lgkm();
    KP(3);
    wload<512>(raw[2], wp, wv, (2 + rot) & 15);
    wait_vm<40>();
    barrier_lgkm();
    VP(3);
    wload<512>(raw[3], wp, wv, (3 + rot) & 15);
    wait_vm<40>();
    barrier_lgkm();
    WO_LDS(5, lst1);
    wait_vm<32>();
    barrier_lgkm();
    WO_LDS(6, lst2);
    wait_vm<24>();
    barrier_lgkm();
    f32x4 acc[8][2];
#pragma unroll
    for (int t = 0; t < 8; ++t) { acc[t][0] = f32x4{0, 0, 0, 0}; acc[t][1] = f32x4{0, 0, 0, 0}; }
    STAMP(3);
    wait_raw<0>(raw[0]); wait_raw<0>(raw[1]); wait_raw<0>(raw[2]); wait_raw<0>(raw[3]);
    barrier_lgkm();
    STAMP(4);
    {
      const char* oimg = smem + LDS_OIMG;
      bf16x8 bf[2];
      bload<32768>(bf, oimg, rot & 15, r16, g);
#pragma unroll
      for (int c = 0; c < 4; ++c) {
        wstep<32768>(raw[c], acc, oimg, bf, (c + 1 + rot) & 15, r16, g);
        __builtin_amdgcn_sched_barrier(0);
        wload<512>(raw[c], wp, wv, (7 + c + rot) & 15);
      }
#pragma unroll
      for (int c = 0; c < 3; ++c) {
        const char* src = smem + (c == 0 ? LDS_X + w4 * 8192 : LDS_RING + (w4 >> 1) * 32768 + (w4 & 1) * 8192 + (c == 2 ? 16384 : 0)) + lane * 16;
        u32x4 tmp[8];
#pragma unroll
        for (int j = 0; j < 8; ++j) tmp[j] = *(const u32x4*)(src + j * 1024);
        wstep<32768>(tmp, acc, oimg, bf, (5 + c + rot) & 15, r16, g);
        __builtin_amdgcn_sched_barrier(0);
      }
#pragma unroll
      for (int c = 7; c < 16; ++c) {
        const int bi = (c - 7) & 3;
        if (c <= 12) wait_raw<24>(raw[bi]); else if (c == 13) wait_raw<16>(raw[bi]); else if (c == 14) wait_raw<8>(raw[bi]); else wait_raw<0>(raw[bi]);
        wstep<32768>(raw[bi], acc, oimg, bf, (c + 1 + rot) & 15, r16, g);
        __builtin_amdgcn_sched_barrier(0);
        if (c + 4 < 16) wload<512>(raw[bi], wp, wv, (c + 4 + rot) & 15);
      }
    }
    STAMP(5);
    {
      const unsigned off = (unsigned)(((b * 8 + 2 * w4 + (g >> 1)) * 8 + hkv) * 4096 + (lane & 31) * 16);
#pragma unroll
      for (int qt = 0; qt < 2; ++qt)
#pragma unroll
        for (int i = 0; i < 4; ++i) {
          u32x4 v;
#pragma unroll
          for (int m = 0; m < 4; ++m) v[m] = cvtpk(acc[2 * m][qt][i], acc[2 * m + 1][qt][i]);
          __builtin_amdgcn_raw_buffer_store_b128(v, xr, off + (qt * 4 + i) * 512, 0, 16);
        }
    }
    wait_vm<0>();
    STAMP(6);
    barrier_lgkm();
  }
  barrier_lgkm();
  STAMP(7);
  {
    const unsigned off = (unsigned)(((b * 8 + hkv) * 8 + (lane >> 5)) * 4096 + wave * 512 + (lane & 31) * 16);
    u32x4 d[4];
#pragma unroll
    for (int p = 0; p < 4; ++p) d[p] = __builtin_amdgcn_raw_buffer_load_b128(xr, off + p * 8192, 0, 16);
    float sum[8];
#pragma unroll
    for (int m = 0; m < 4; ++m) {
      float lo = 0.f, hi = 0.f;
#pragma unroll
      for (int p = 0; p < 4; ++p) { lo += __uint_as_float(d[p][m] << 16); hi += __uint_as_float(d[p][m] & 0xffff0000u); }
      sum[2 * m] = lo; sum[2 * m + 1] = hi;
    }
#pragma unroll
    for (int e = 0; e < 8; ++e) {
      auto rr = __builtin_amdgcn_permlane32_swap(__float_as_uint(sum[e]), __float_as_uint(sum[e]), false, false);
      sum[e] = __uint_as_float(rr[0]) + __uint_as_float(rr[1]);
    }
    if (lane < 32) {
      u32x4 v;
#pragma unroll
      for (int m = 0; m < 4; ++m) v[m] = cvtpk(sum[2 * m], sum[2 * m + 1]);
      const int q = (lane & 15) + 16 * (wave >> 2), n = 64 * hkv + 32 * ((lane >> 4) & 1) + 8 * (wave & 3);
      *(u32x4*)(out + (size_t)b * (SQ * DM) + q * DM + n) = v;
    }
  }
  if (wave == 0) {
    gu32* tokp = (gu32*)(ws + WS_TOK + (size_t)b * 128) + (lane & 7);
    gu32* outbox = (gu32*)(ws + WS_FLAG + (size_t)(b * 8 + (lane & 7)) * 128) + hkv;
    const unsigned t2 = __hip_atomic_load(tokp, RLX_AGENT);
    if (lane < 8 && t2 != tok) __hip_atomic_store(outbox, t2, RLX_AGENT);
  }
  STAMP(8);
  STAMP_FLUSH;
}

extern "C" void kernel_launch(void* const* d_in, const int* in_sizes, int n_in, void* d_out, int out_size, void* d_ws,
                              size_t ws_size, hipStream_t stream) {
  const u16* x = (const u16*)d_in[0];
  const u16* kc = (const u16*)d_in[1];
  const u16* vc = (const u16*)d_in[2];
  const u16* wq = (const u16*)d_in[3];
  const u16* wo = (const u16*)d_in[4];
  (void)hipFuncSetAttribute((const void*)fused_kernel, hipFuncAttributeMaxDynamicSharedMemorySize, LDS_TOTAL);
  fused_kernel<<<256, 512, LDS_TOTAL, stream>>>(x, kc, vc, wq, wo, (char*)d_ws, (u16*)d_out);
}
```

```cpp
#include <hip/hip_runtime.h>
#include <hip/hip_bf16.h>

typedef __attribute__((ext_vector_type(8))) short bf16x8;
typedef __attribute__((ext_vector_type(4))) short s16x4;
typedef __attribute__((ext_vector_type(4))) float f32x4;
typedef __attribute__((ext_vector_type(16))) float f32x16;
typedef __attribute__((ext_vector_type(4))) unsigned u32x4;
typedef __attribute__((ext_vector_type(2))) unsigned u32x2;
typedef unsigned short u16;

#define DI __device__ __forceinline__
#define LDS3 __attribute__((address_space(3)))

constexpr int B_ = 32, SQ = 32, DM = 512, HQ = 32, HKV = 8, SK = 512, DH = 128;
constexpr int LDS_RING = 0;
constexpr int LDS_X = 131072;
constexpr int LDS_TOTAL = 163840;
constexpr int LDS_OIMG = LDS_RING + 65536;

#define STAMP(i) do {} while (0)
#define STAMP_DECL
#define STAMP_FLUSH do {} while (0)

typedef __attribute__((ext_vector_type(2))) __bf16 bf16x2_t;
typedef __attribute__((ext_vector_type(2))) float f32x2;
DI unsigned cvtpk(float lo, float hi) {
  f32x2 v = {lo, hi};
  return __builtin_bit_cast(unsigned, __builtin_convertvector(v, bf16x2_t));
}

DI void barrier_lgkm() { asm volatile("s_waitcnt lgkmcnt(0)\n\ts_barrier" ::: "memory"); }
DI void barrier_raw() { asm volatile("s_barrier" ::: "memory"); }

DI void glds16(const void* sbase, unsigned voff, unsigned lds_dst) {
  unsigned keep;
  asm volatile("s_mov_b32 %0, m0\n\ts_mov_b32 m0, %3\n\ts_nop 0\n\tglobal_load_lds_dwordx4 %1, %2\n\ts_mov_b32 m0, %0"
               : "=&s"(keep) : "v"(voff), "s"(sbase), "s"(lds_dst) : "memory");
}
DI void glds16_nt(const void* sbase, unsigned voff, unsigned lds_dst) {
  unsigned keep;
  asm volatile("s_mov_b32 %0, m0\n\ts_mov_b32 m0, %3\n\ts_nop 0\n\tglobal_load_lds_dwordx4 %1, %2 nt\n\ts_mov_b32 m0, %0"
               : "=&s"(keep) : "v"(voff), "s"(sbase), "s"(lds_dst) : "memory");
}
DI void gload16(u32x4& dst, const void* sbase, unsigned voff) {
  asm volatile("global_load_dwordx4 %0, %1, %2" : "=v"(dst) : "v"(voff), "s"(sbase) : "memory");
}
template <int N>
DI void wait_raw(u32x4 (&r)[8]) {
  asm volatile("s_waitcnt vmcnt(%8)"
               : "+v"(r[0]), "+v"(r[1]), "+v"(r[2]), "+v"(r[3]), "+v"(r[4]), "+v"(r[5]), "+v"(r[6]), "+v"(r[7]) : "i"(N) : "memory");
}
template <int N> DI void wait_vm() { asm volatile("s_waitcnt vmcnt(%0)" :: "i"(N) : "memory"); }

template <int RS>
DI void wload(u32x4 (&raw)[8], const u16* p, unsigned voff, int ks) {
  const u16* q = p + (size_t)(32 * ks) * RS;
#pragma unroll
  for (int j = 0; j < 8; ++j) gload16(raw[j], q + (size_t)j * RS, voff);
}
template <int QOFF>
DI void bload(bf16x8 (&bf)[2], const char* bimg, int ks, int r16, int g) {
  const unsigned boff = r16 * 1024 + (((4 * ks + g) ^ r16) << 4);
  bf[0] = *(const bf16x8*)(bimg + boff);
  bf[1] = *(const bf16x8*)(bimg + boff + QOFF);
}
template <int QOFF = 16384>
DI void wstep(const u32x4 (&raw)[8], f32x4 (&acc)[8][2], const char* bimg, bf16x8 (&bf)[2], int ksn, int r16, int g) {
  const bf16x8 b0 = bf[0], b1 = bf[1];
  bload<QOFF>(bf, bimg, ksn, r16, g);
#pragma unroll
  for (int c = 0; c < 4; ++c) {
    u32x4 f0, f1;
#pragma unroll
    for (int m = 0; m < 4; ++m) {
      f0[m] = __builtin_amdgcn_perm(raw[2 * m + 1][c], raw[2 * m][c], 0x05040100u);
      f1[m] = __builtin_amdgcn_perm(raw[2 * m + 1][c], raw[2 * m][c], 0x07060302u);
    }
    const bf16x8 a0 = __builtin_bit_cast(bf16x8, f0), a1 = __builtin_bit_cast(bf16x8, f1);
    acc[2 * c][0] = __builtin_amdgcn_mfma_f32_16x16x32_bf16(a0, b0, acc[2 * c][0], 0, 0, 0);
    acc[2 * c][1] = __builtin_amdgcn_mfma_f32_16x16x32_bf16(a0, b1, acc[2 * c][1], 0, 0, 0);
    acc[2 * c + 1][0] = __builtin_amdgcn_mfma_f32_16x16x32_bf16(a1, b0, acc[2 * c + 1][0], 0, 0, 0);
    acc[2 * c + 1][1] = __builtin_amdgcn_mfma_f32_16x16x32_bf16(a1, b1, acc[2 * c + 1][1], 0, 0, 0);
  }
}
template <int RS, bool TRIG>
DI void proj_loop(u32x4 (&raw)[4][8], f32x4 (&acc)[8][2], const u16* wp, unsigned wv, const char* bimg, int rot, int r16, int g, int stag) {
  bf16x8 bf[2];
  bload<16384>(bf, bimg, rot & 15, r16, g);
#pragma unroll 1
  for (int it = 0; it < 3; ++it) {
#pragma unroll
    for (int u = 0; u < 4; ++u) {
      const int s = 4 * it + u;
      wait_raw<16>(raw[u]);
      wload<RS>(raw[(u + 3) & 3], wp, wv, (s + 3 + rot) & 15);
      __builtin_amdgcn_sched_barrier(0);
      wstep(raw[u], acc, bimg, bf, (s + 1 + rot) & 15, r16, g);
      __builtin_amdgcn_sched_barrier(0);
      if (TRIG) { if (u == stag) barrier_raw(); }
    }
  }
  wait_raw<16>(raw[0]);
  wload<RS>(raw[3], wp, wv, (15 + rot) & 15);
  __builtin_amdgcn_sched_barrier(0);
  wstep(raw[0], acc, bimg, bf, (13 + rot) & 15, r16, g);
  wait_raw<16>(raw[1]); wstep(raw[1], acc, bimg, bf, (14 + rot) & 15, r16, g);
  wait_raw<8>(raw[2]);  wstep(raw[2], acc, bimg, bf, (15 + rot) & 15, r16, g);
  wait_raw<0>(raw[3]);  wstep(raw[3], acc, bimg, bf, (15 + rot) & 15, r16, g);
}

typedef __attribute__((address_space(1))) unsigned gu32;
#define RLX_AGENT __ATOMIC_RELAXED, __HIP_MEMORY_SCOPE_AGENT
constexpr size_t WS_XBUF = 0, XBUF_BYTES = (size_t)32 * 8 * 8 * 4096;
constexpr size_t WS_FLAG = XBUF_BYTES;
constexpr size_t WS_TOK = WS_FLAG + 256 * 128;

__global__ __launch_bounds__(512, 2) void fused_kernel(const u16* x, const u16* kc, const u16* vc, const u16* wq, const u16* wo,
                                                       char* ws, u16* out
) {
  extern __shared__ __attribute__((aligned(16))) char smem[];
  const int tid = threadIdx.x, lane = tid & 63;
  const int wave = __builtin_amdgcn_readfirstlane(tid >> 6);
  const int hkv = blockIdx.x & 7, b = blockIdx.x >> 3;
  const int w4 = wave & 3;
  const bool isW = wave >= 4;
  const int r16 = lane & 15, g = lane >> 4;
  const int r32 = lane & 31, h = lane >> 5;
  STAMP_DECL
  STAMP(0);
  const char* ximg = smem + LDS_X;
  const int stag = b & 3;
  const int rot = b & 15;
  unsigned tok = 0;
  const __amdgpu_buffer_rsrc_t xr = __builtin_amdgcn_make_buffer_rsrc(ws + WS_XBUF, 0, (int)XBUF_BYTES, 0x00020000);
  const unsigned sbase = (unsigned)(size_t)smem;
  const u16* kp = kc + (size_t)(b * HKV + hkv) * SK * DH;
  const u16* vp = vc + (size_t)(b * HKV + hkv) * SK * DH;
#define K_ONE(t) do { \
    int ln_ = lane; asm volatile("" : "+v"(ln_)); \
    const int r_ = ln_ & 15, g_ = ln_ >> 4; \
    _Pragma("unroll") for (int j = 0; j < 4; ++j) { const int row = 16 * w4 + 4 * j + g_; \
      glds16_nt(kp + (t) * 64 * DH, (row * DH + 8 * (r_ ^ (row & 15))) * 2, sbase + LDS_RING + ((t) & 3) * 32768 + (16 * w4 + 4 * j) * 256); } \
  } while (0)
#define V_ONE(t) do { \
    int ln_ = lane; asm volatile("" : "+v"(ln_)); \
    const int r_ = ln_ & 15, g_ = ln_ >> 4; \
    _Pragma("unroll") for (int j = 0; j < 4; ++j) { const int row = 16 * w4 + 4 * j + g_; \
      glds16_nt(vp + (t) * 64 * DH, (row * DH + 8 * (r_ ^ (((row & 3) << 2) | ((row >> 2) & 3)))) * 2, sbase + LDS_RING + ((t) & 3) * 32768 + 16384 + (16 * w4 + 4 * j) * 256); } \
  } while (0)
#define KP(j) do { K_ONE(2 * (j)); K_ONE(2 * (j) + 1); } while (0)
#define VP(j) do { V_ONE(2 * (j)); V_ONE(2 * (j) + 1); } while (0)

  if (!isW) {
    {
      const u16* xp = x + (size_t)b * SQ * DM;
#pragma unroll
      for (int i = 0; i < 8; ++i) {
        const int row = 8 * w4 + i;
        const int c = (lane & ~15) | ((lane & 15) ^ (row & 15));
        glds16(xp, (unsigned)(row * DM + c * 8) * 2, sbase + LDS_X + row * 1024);
      }
    }
    bf16x8 qf[8];
    {
      const u16* wp = wq + hkv * 512 + w4 * 128;
      const unsigned wv = (unsigned)(8 * g * 4096 + r16 * 8) * 2;
      u32x4 raw[4][8];
      wload<4096>(raw[0], wp, wv, (0 + rot) & 15);
      f32x4 acc[8][2];
#pragma unroll
      for (int t = 0; t < 8; ++t) { acc[t][0] = f32x4{0, 0, 0, 0}; acc[t][1] = f32x4{0, 0, 0, 0}; }
      wait_vm<8>();
      barrier_lgkm();
      wload<4096>(raw[1], wp, wv, (1 + rot) & 15);
      wload<4096>(raw[2], wp, wv, (2 + rot) & 15);
      STAMP(1);
      proj_loop<4096, true>(raw, acc, wp, wv, ximg, rot, r16, g, stag);
#pragma unroll
      for (int i = 0; i < 4; ++i) {
        u32x4 a, bb;
#pragma unroll
        for (int m = 0; m < 4; ++m) {
          unsigned a_ = cvtpk(acc[2 * m][0][i], acc[2 * m + 1][0][i]);
          unsigned b_ = cvtpk(acc[2 * m][1][i], acc[2 * m + 1][1][i]);
          auto r = __builtin_amdgcn_permlane16_swap(a_, b_, false, false);
          a[m] = r[0]; bb[m] = r[1];
        }
        qf[i] = __builtin_bit_cast(bf16x8, a);
        qf[4 + i] = __builtin_bit_cast(bf16x8, bb);
      }
    }
    STAMP(2);
    unsigned kaddr[8], vaddr[8];
#pragma unroll
    for (int s = 0; s < 8; ++s) kaddr[s] = r32 * 256 + 16 * ((8 * h + s) ^ (r32 & 15));
    {
      const int blk = (lane >> 4) & 1, qq = (lane & 15) >> 2, p = lane & 3;
#pragma unroll
      for (int dt = 0; dt < 4; ++dt)
#pragma unroll
        for (int e = 0; e < 2; ++e)
          vaddr[dt * 2 + e] = (8 * e + 4 * h + qq) * 256 + 16 * (4 * (dt ^ qq) + ((2 * blk + (p >> 1)) ^ (2 * e + h))) + 8 * (p & 1) + 16384;
    }
    const float csc = 0.08838834764831845f * 1.4426950408889634f;
    float m_run = -__builtin_inff(), l_run = 0.f;
    f32x16 o[4];
#pragma unroll
    for (int dt = 0; dt < 4; ++dt)
#pragma unroll
      for (int i = 0; i < 16; ++i) o[dt][i] = 0.f;

    f32x16 lacc;
#pragma unroll
    for (int i = 0; i < 16; ++i) lacc[i] = 0.f;
    bf16x8 ones;
#pragma unroll
    for (int i = 0; i < 8; ++i) ones[i] = (short)0x3F80;
#pragma unroll 1
    for (int j = 0; j < 4; ++j) {
      barrier_lgkm();
      const char* slot = smem + LDS_RING + (j & 1) * 65536;
      f32x16 sc[4];
      bf16x8 kf[2][4];
      float mxa = m_run, mxb = m_run;
#define K_LOAD(gi, buf) do { _Pragma("unroll") for (int s_ = 0; s_ < 4; ++s_) \
        kf[buf][s_] = *(const bf16x8*)(slot + kaddr[2 * ((gi) & 3) + (s_ >> 1)] + (s_ & 1) * 8192 + ((gi) >> 2) * 32768); } while (0)
      K_LOAD(0, 0);
#pragma unroll
      for (int gi = 0; gi < 8; ++gi) {
        const int pr = gi >> 2;
        __builtin_amdgcn_sched_barrier(0);
        if (gi < 7) K_LOAD(gi + 1, (gi + 1) & 1);
        if ((gi & 3) == 0) {
#pragma unroll
          for (int i = 0; i < 16; ++i) { sc[2 * pr][i] = 0.f; sc[2 * pr + 1][i] = 0.f; }
        }
#pragma unroll
        for (int s = 0; s < 4; ++s)
          sc[2 * pr + (s & 1)] = __builtin_amdgcn_mfma_f32_32x32x16_bf16(kf[gi & 1][s], qf[2 * (gi & 3) + (s >> 1)], sc[2 * pr + (s & 1)], 0, 0, 0);
        if (gi == 5 || gi == 6) {
#pragma unroll
          for (int i = 0; i < 8; ++i) { mxa = fmaxf(mxa, sc[gi - 5][i]); mxb = fmaxf(mxb, sc[gi - 5][8 + i]); }
        }
      }
      bf16x8 pf[2], vf[2][4];
#define V_LOAD(ks, buf) do { \
        const char* vb_ = slot + ((ks) >> 2) * 32768 + 4096 * ((ks) & 3); \
        _Pragma("unroll") for (int dt = 0; dt < 4; ++dt) { \
          const s16x4 lo = __builtin_amdgcn_ds_read_tr16_b64_v4i16((LDS3 s16x4*)(vb_ + vaddr[dt * 2 + 0])); \
          const s16x4 hi = __builtin_amdgcn_ds_read_tr16_b64_v4i16((LDS3 s16x4*)(vb_ + vaddr[dt * 2 + 1])); \
          vf[buf][dt] = __builtin_shufflevector(lo, hi, 0, 1, 2, 3, 4, 5, 6, 7); } } while (0)
#define P_FMA(ks) do { const int u_ = (ks) >> 1, r0_ = 8 * ((ks) & 1); \
        _Pragma("unroll") for (int m = 0; m < 8; ++m) sc[u_][r0_ + m] = fmaf(sc[u_][r0_ + m], csc, nm); } while (0)
#define P_EXP(ks, buf) do { \
        const int u_ = (ks) >> 1, r0_ = 8 * ((ks) & 1); u32x4 pw_; \
        _Pragma("unroll") for (int m = 0; m < 4; ++m) \
          pw_[m] = cvtpk(__builtin_amdgcn_exp2f(sc[u_][r0_ + 2 * m]), __builtin_amdgcn_exp2f(sc[u_][r0_ + 2 * m + 1])); \
        pf[buf] = __builtin_bit_cast(bf16x8, pw_); } while (0)
      barrier_lgkm();
      V_LOAD(0, 0);
#pragma unroll
      for (int i = 0; i < 16; ++i) { mxa = fmaxf(mxa, sc[2][i]); mxb = fmaxf(mxb, sc[3][i]); }
      float mx = fmaxf(mxa, mxb);
      {
        auto rr = __builtin_amdgcn_permlane32_swap(__float_as_uint(mx), __float_as_uint(mx), false, false);
        mx = fmaxf(__uint_as_float(rr[0]), __uint_as_float(rr[1]));
      }
      const float alpha = __builtin_amdgcn_exp2f((m_run - mx) * csc);
      const float nm = -mx * csc;
      m_run = mx;
      P_FMA(0);
      P_FMA(1);
      P_EXP(0, 0);
      lacc[0] *= alpha;
#pragma unroll
      for (int ks = 0; ks < 8; ++ks) {
        __builtin_amdgcn_sched_barrier(0);
        if (ks < 7) { V_LOAD(ks + 1, (ks + 1) & 1); }
        lacc = __builtin_amdgcn_mfma_f32_32x32x16_bf16(ones, pf[ks & 1], lacc, 0, 0, 0);
#pragma unroll
        for (int dt = 0; dt < 4; ++dt) {
          if (ks == 0) {
#pragma unroll
            for (int i = 0; i < 16; ++i) o[dt][i] *= alpha;
          }
          o[dt] = __builtin_amdgcn_mfma_f32_32x32x16_bf16(vf[ks & 1][dt], pf[ks & 1], o[dt], 0, 0, 0);
        }
        if (ks < 7) { P_EXP(ks + 1, (ks + 1) & 1); }
        if (ks < 6) { P_FMA(ks + 2); }
        if (ks > 0 && ks < 6) {
#pragma unroll
          for (int gq = 0; gq < 4; ++gq) {
            __builtin_amdgcn_sched_group_barrier(0x008, 1, 0);
            __builtin_amdgcn_sched_group_barrier(0x100, 2, 0);
            __builtin_amdgcn_sched_group_barrier(0x400, 2, 0);
            __builtin_amdgcn_sched_group_barrier(0x002, 2, 0);
          }
          __builtin_amdgcn_sched_group_barrier(0x008, 1, 0);
          __builtin_amdgcn_sched_group_barrier(0x002, 4, 0);
        }
      }
    }
    l_run = lacc[0];
    STAMP(3);
    const float inv = 1.0f / l_run;
#pragma unroll
    for (int dt = 0; dt < 4; ++dt)
#pragma unroll
      for (int a = 0; a < 4; ++a) {
        u32x2 v;
        v[0] = cvtpk(o[dt][4 * a] * inv, o[dt][4 * a + 1] * inv);
        v[1] = cvtpk(o[dt][4 * a + 2] * inv, o[dt][4 * a + 3] * inv);
        const int c = 16 * w4 + 4 * dt + a;
        *(u32x2*)(smem + LDS_OIMG + (r32 & 15) * 1024 + (r32 >> 4) * 32768 + 16 * (c ^ (r32 & 15)) + 8 * h) = v;
      }
    barrier_lgkm();
    STAMP(4);
    if (wave == 0) {
      gu32* tokp = (gu32*)(ws + WS_TOK + (size_t)b * 128) + (lane & 7);
      gu32* inbox = (gu32*)(ws + WS_FLAG + (size_t)(b * 8 + hkv) * 128) + (lane & 7);
      gu32* outbox = (gu32*)(ws + WS_FLAG + (size_t)(b * 8 + (lane & 7)) * 128) + hkv;
      tok = __hip_atomic_load(tokp, RLX_AGENT);
      const unsigned mytok = __builtin_amdgcn_readlane(tok, hkv);
      barrier_lgkm();
      if (lane < 8) __hip_atomic_store(outbox, tok, RLX_AGENT);
      for (unsigned spin = 0; spin < (1u << 20); ++spin) {
        const unsigned f = __hip_atomic_load(inbox, RLX_AGENT);
        const unsigned t2 = __hip_atomic_load(tokp, RLX_AGENT);
        if (lane < 8 && t2 != tok) { tok = t2; __hip_atomic_store(outbox, tok, RLX_AGENT); }
        if (__all(f == mytok)) break;
        __builtin_amdgcn_s_sleep(1);
      }
      asm volatile("" ::: "memory");
    } else {
      barrier_lgkm();
    }
  } else {
    if (wave == 5) {
      const char* p = lane == 0 ? (const char*)kp : lane == 1 ? (const char*)vp : lane == 2 ? (const char*)(wo + (size_t)hkv * 512 * 512)
                    : lane == 3 ? (const char*)(wo + (size_t)hkv * 512 * 512 + 131072) : lane == 4 ? ws + WS_XBUF + (size_t)b * 262144
                    : lane == 5 ? (const char*)(out + (size_t)b * SQ * DM) : lane == 6 ? (const char*)(kp + 65535) : (const char*)(vp + 65535);
      if (lane < 8) { const unsigned v = *(const unsigned short*)p; asm volatile("" :: "v"(v)); }
    }
    barrier_lgkm();
    STAMP(1);
    if (wave == 4) {
      gu32* inbox = (gu32*)(ws + WS_FLAG + (size_t)(b * 8 + hkv) * 128);
      gu32* mytokp = (gu32*)(ws + WS_TOK + (size_t)b * 128) + hkv;
      const unsigned v = __hip_atomic_load(lane < 8 ? inbox + lane : mytokp, RLX_AGENT);
      unsigned t = __builtin_amdgcn_readlane(v, 8) + 1;
      while (__any(v == t)) ++t;
      if (lane == 0) __hip_atomic_store(mytokp, t, RLX_AGENT);
    }
    const u16* wp = wo + (size_t)(hkv * 512) * 512 + w4 * 128;
    const unsigned wv = (unsigned)(8 * g * 512 + r16 * 8) * 2;
    u32x4 raw[4][8];
    const unsigned lst0 = sbase + LDS_X + w4 * 8192;
    const unsigned lst1 = sbase + LDS_RING + (w4 >> 1) * 32768 + (w4 & 1) * 8192;
    const unsigned lst2 = lst1 + 16384;
#define WO_LDS(idx, dst) do { const u16* q_ = wp + (size_t)(32 * (((idx) + rot) & 15)) * 512; \
      _Pragma("unroll") for (int j = 0; j < 8; ++j) glds16(q_ + j * 512, wv, (dst) + j * 1024); } while (0)
    barrier_raw();
    KP(0);
    barrier_raw();
    VP(0);
    barrier_raw();
    KP(1);
    wait_vm<16>();
    barrier_lgkm();
    VP(1);
    WO_LDS(4, lst0);
    wait_vm<24>();
    barrier_lgkm();
    KP(2);
    wload<512>(raw[0], wp, wv, (0 + rot) & 15);
    wait_vm<32>();
    barrier_lgkm();
    VP(2);
    wload<512>(raw[1], wp, wv, (1 + rot) & 15);
    wait_vm<40>();
    barrier_lgkm();
    KP(3);
    wload<512>(raw[2], wp, wv, (2 + rot) & 15);
    wait_vm<40>();
    barrier_lgkm();
    VP(3);
    wload<512>(raw[3], wp, wv, (3 + rot) & 15);
    wait_vm<40>();
    barrier_lgkm();
    WO_LDS(5, lst1);
    wait_vm<32>();
    barrier_lgkm();
    WO_LDS(6, lst2);
    wait_vm<24>();
    barrier_lgkm();
    f32x4 acc[8][2];
#pragma unroll
    for (int t = 0; t < 8; ++t) { acc[t][0] = f32x4{0, 0, 0, 0}; acc[t][1] = f32x4{0, 0, 0, 0}; }
    STAMP(3);
    wait_raw<0>(raw[0]); wait_raw<0>(raw[1]); wait_raw<0>(raw[2]); wait_raw<0>(raw[3]);
    barrier_lgkm();
    STAMP(4);
    {
      const char* oimg = smem + LDS_OIMG;
      bf16x8 bf[2];
      bload<32768>(bf, oimg, rot & 15, r16, g);
#pragma unroll
      for (int c = 0; c < 4; ++c) {
        wstep<32768>(raw[c], acc, oimg, bf, (c + 1 + rot) & 15, r16, g);
        __builtin_amdgcn_sched_barrier(0);
        wload<512>(raw[c], wp, wv, (7 + c + rot) & 15);
      }
#pragma unroll
      for (int c = 0; c < 3; ++c) {
        const char* src = smem + (c == 0 ? LDS_X + w4 * 8192 : LDS_RING + (w4 >> 1) * 32768 + (w4 & 1) * 8192 + (c == 2 ? 16384 : 0)) + lane * 16;
        u32x4 tmp[8];
#pragma unroll
        for (int j = 0; j < 8; ++j) tmp[j] = *(const u32x4*)(src + j * 1024);
        wstep<32768>(tmp, acc, oimg, bf, (5 + c + rot) & 15, r16, g);
        __builtin_amdgcn_sched_barrier(0);
      }
#pragma unroll
      for (int c = 7; c < 16; ++c) {
        const int bi = (c - 7) & 3;
        if (c <= 12) wait_raw<24>(raw[bi]); else if (c == 13) wait_raw<16>(raw[bi]); else if (c == 14) wait_raw<8>(raw[bi]); else wait_raw<0>(raw[bi]);
        wstep<32768>(raw[bi], acc, oimg, bf, (c + 1 + rot) & 15, r16, g);
        __builtin_amdgcn_sched_barrier(0);
        if (c + 4 < 16) wload<512>(raw[bi], wp, wv, (c + 4 + rot) & 15);
      }
    }
    STAMP(5);
    {
      const unsigned off = (unsigned)(((b * 8 + 2 * w4 + (g >> 1)) * 8 + hkv) * 4096 + (lane & 31) * 16);
#pragma unroll
      for (int qt = 0; qt < 2; ++qt)
#pragma unroll
        for (int i = 0; i < 4; ++i) {
          u32x4 v;
#pragma unroll
          for (int m = 0; m < 4; ++m) v[m] = cvtpk(acc[2 * m][qt][i], acc[2 * m + 1][qt][i]);
          __builtin_amdgcn_raw_buffer_store_b128(v, xr, off + (qt * 4 + i) * 512, 0, 16);
        }
    }
    wait_vm<0>();
    STAMP(6);
    barrier_lgkm();
  }
  barrier_lgkm();
  STAMP(7);
  {
    const unsigned off = (unsigned)(((b * 8 + hkv) * 8 + (lane >> 5)) * 4096 + wave * 512 + (lane & 31) * 16);
    u32x4 d[4];
#pragma unroll
    for (int p = 0; p < 4; ++p) d[p] = __builtin_amdgcn_raw_buffer_load_b128(xr, off + p * 8192, 0, 16);
    float sum[8];
#pragma unroll
    for (int m = 0; m < 4; ++m) {
      float lo = 0.f, hi = 0.f;
#pragma unroll
      for (int p = 0; p < 4; ++p) { lo += __uint_as_float(d[p][m] << 16); hi += __uint_as_float(d[p][m] & 0xffff0000u); }
      sum[2 * m] = lo; sum[2 * m + 1] = hi;
    }
#pragma unroll
    for (int e = 0; e < 8; ++e) {
      auto rr = __builtin_amdgcn_permlane32_swap(__float_as_uint(sum[e]), __float_as_uint(sum[e]), false, false);
      sum[e] = __uint_as_float(rr[0]) + __uint_as_float(rr[1]);
    }
    if (lane < 32) {
      u32x4 v;
#pragma unroll
      for (int m = 0; m < 4; ++m) v[m] = cvtpk(sum[2 * m], sum[2 * m + 1]);
      const int q = (lane & 15) + 16 * (wave >> 2), n = 64 * hkv + 32 * ((lane >> 4) & 1) + 8 * (wave & 3);
      *(u32x4*)(out + (size_t)b * (SQ * DM) + q * DM + n) = v;
    }
  }
  if (wave == 0) {
    gu32* tokp = (gu32*)(ws + WS_TOK + (size_t)b * 128) + (lane & 7);
    gu32* outbox = (gu32*)(ws + WS_FLAG + (size_t)(b * 8 + (lane & 7)) * 128) + hkv;
    const unsigned t2 = __hip_atomic_load(tokp, RLX_AGENT);
    if (lane < 8 && t2 != tok) __hip_atomic_store(outbox, t2, RLX_AGENT);
  }
  STAMP(8);
  STAMP_FLUSH;
}

extern "C" void kernel_launch(void* const* d_in, const int* in_sizes, int n_in, void* d_out, int out_size, void* d_ws,
                              size_t ws_size, hipStream_t stream) {
  const u16* x = (const u16*)d_in[0];
  const u16* kc = (const u16*)d_in[1];
  const u16* vc = (const u16*)d_in[2];
  const u16* wq = (const u16*)d_in[3];
  const u16* wo = (const u16*)d_in[4];
  (void)hipFuncSetAttribute((const void*)fused_kernel, hipFuncAttributeMaxDynamicSharedMemorySize, LDS_TOTAL);
  fused_kernel<<<256, 512, LDS_TOTAL, stream>>>(x, kc, vc, wq, wo, (char*)d_ws, (u16*)d_out);
}
```

```cpp
#include <hip/hip_runtime.h>
#include <hip/hip_bf16.h>

typedef __attribute__((ext_vector_type(8))) short bf16x8;
typedef __attribute__((ext_vector_type(4))) short s16x4;
typedef __attribute__((ext_vector_type(4))) float f32x4;
typedef __attribute__((ext_vector_type(16))) float f32x16;
typedef __attribute__((ext_vector_type(4))) unsigned u32x4;
typedef __attribute__((ext_vector_type(2))) unsigned u32x2;
typedef unsigned short u16;

#define DI __device__ __forceinline__
#define LDS3 __attribute__((address_space(3)))

constexpr int B_ = 32, SQ = 32, DM = 512, HQ = 32, HKV = 8, SK = 512, DH = 128;
constexpr int LDS_RING = 0;
constexpr int LDS_X = 131072;
constexpr int LDS_TOTAL = 163840;
constexpr int LDS_OIMG = LDS_RING + 65536;


typedef __attribute__((ext_vector_type(2))) __bf16 bf16x2_t;
typedef __attribute__((ext_vector_type(2))) float f32x2;
DI unsigned cvtpk(float lo, float hi) {
  f32x2 v = {lo, hi};
  return __builtin_bit_cast(unsigned, __builtin_convertvector(v, bf16x2_t));
}

DI void barrier_lgkm() { asm volatile("s_waitcnt lgkmcnt(0)\n\ts_barrier" ::: "memory"); }
DI void barrier_raw() { asm volatile("s_barrier" ::: "memory"); }

DI void glds16(const void* sbase, unsigned voff, unsigned lds_dst) {
  unsigned keep;
  asm volatile("s_mov_b32 %0, m0\n\ts_mov_b32 m0, %3\n\ts_nop 0\n\tglobal_load_lds_dwordx4 %1, %2\n\ts_mov_b32 m0, %0"
               : "=&s"(keep) : "v"(voff), "s"(sbase), "s"(lds_dst) : "memory");
}
DI void glds16_nt(const void* sbase, unsigned voff, unsigned lds_dst) {
  unsigned keep;
  asm volatile("s_mov_b32 %0, m0\n\ts_mov_b32 m0, %3\n\ts_nop 0\n\tglobal_load_lds_dwordx4 %1, %2 nt\n\ts_mov_b32 m0, %0"
               : "=&s"(keep) : "v"(voff), "s"(sbase), "s"(lds_dst) : "memory");
}
DI void gload16(u32x4& dst, const void* sbase, unsigned voff) {
  asm volatile("global_load_dwordx4 %0, %1, %2" : "=v"(dst) : "v"(voff), "s"(sbase) : "memory");
}
template <int N>
DI void wait_raw(u32x4 (&r)[8]) {
  asm volatile("s_waitcnt vmcnt(%8)"
               : "+v"(r[0]), "+v"(r[1]), "+v"(r[2]), "+v"(r[3]), "+v"(r[4]), "+v"(r[5]), "+v"(r[6]), "+v"(r[7]) : "i"(N) : "memory");
}
template <int N> DI void wait_vm() { asm volatile("s_waitcnt vmcnt(%0)" :: "i"(N) : "memory"); }

template <int RS>
DI void wload(u32x4 (&raw)[8], const u16* p, unsigned voff, int ks) {
  const u16* q = p + (size_t)(32 * ks) * RS;
#pragma unroll
  for (int j = 0; j < 8; ++j) gload16(raw[j], q + (size_t)j * RS, voff);
}
template <int QOFF>
DI void bload(bf16x8 (&bf)[2], const char* bimg, int ks, int r16, int g) {
  const unsigned boff = r16 * 1024 + (((4 * ks + g) ^ r16) << 4);
  bf[0] = *(const bf16x8*)(bimg + boff);
  bf[1] = *(const bf16x8*)(bimg + boff + QOFF);
}
template <int QOFF = 16384>
DI void wstep(const u32x4 (&raw)[8], f32x4 (&acc)[8][2], const char* bimg, bf16x8 (&bf)[2], int ksn, int r16, int g) {
  const bf16x8 b0 = bf[0], b1 = bf[1];
  bload<QOFF>(bf, bimg, ksn, r16, g);
#pragma unroll
  for (int c = 0; c < 4; ++c) {
    u32x4 f0, f1;
#pragma unroll
    for (int m = 0; m < 4; ++m) {
      f0[m] = __builtin_amdgcn_perm(raw[2 * m + 1][c], raw[2 * m][c], 0x05040100u);
      f1[m] = __builtin_amdgcn_perm(raw[2 * m + 1][c], raw[2 * m][c], 0x07060302u);
    }
    const bf16x8 a0 = __builtin_bit_cast(bf16x8, f0), a1 = __builtin_bit_cast(bf16x8, f1);
    acc[2 * c][0] = __builtin_amdgcn_mfma_f32_16x16x32_bf16(a0, b0, acc[2 * c][0], 0, 0, 0);
    acc[2 * c][1] = __builtin_amdgcn_mfma_f32_16x16x32_bf16(a0, b1, acc[2 * c][1], 0, 0, 0);
    acc[2 * c + 1][0] = __builtin_amdgcn_mfma_f32_16x16x32_bf16(a1, b0, acc[2 * c + 1][0], 0, 0, 0);
    acc[2 * c + 1][1] = __builtin_amdgcn_mfma_f32_16x16x32_bf16(a1, b1, acc[2 * c + 1][1], 0, 0, 0);
  }
}
template <int RS, bool TRIG>
DI void proj_loop(u32x4 (&raw)[4][8], f32x4 (&acc)[8][2], const u16* wp, unsigned wv, const char* bimg, int rot, int r16, int g, int stag) {
  bf16x8 bf[2];
  bload<16384>(bf, bimg, rot & 15, r16, g);
#pragma unroll 1
  for (int it = 0; it < 3; ++it) {
#pragma unroll
    for (int u = 0; u < 4; ++u) {
      const int s = 4 * it + u;
      wait_raw<16>(raw[u]);
      wload<RS>(raw[(u + 3) & 3], wp, wv, (s + 3 + rot) & 15);
      __builtin_amdgcn_sched_barrier(0);
      wstep(raw[u], acc, bimg, bf, (s + 1 + rot) & 15, r16, g);
      __builtin_amdgcn_sched_barrier(0);
      if (TRIG) { if (u == stag) barrier_raw(); }
    }
  }
  wait_raw<16>(raw[0]);
  wload<RS>(raw[3], wp, wv, (15 + rot) & 15);
  __builtin_amdgcn_sched_barrier(0);
  wstep(raw[0], acc, bimg, bf, (13 + rot) & 15, r16, g);
  wait_raw<16>(raw[1]); wstep(raw[1], acc, bimg, bf, (14 + rot) & 15, r16, g);
  wait_raw<8>(raw[2]);  wstep(raw[2], acc, bimg, bf, (15 + rot) & 15, r16, g);
  wait_raw<0>(raw[3]);  wstep(raw[3], acc, bimg, bf, (15 + rot) & 15, r16, g);
}

typedef __attribute__((address_space(1))) unsigned gu32;
#define RLX_AGENT __ATOMIC_RELAXED, __HIP_MEMORY_SCOPE_AGENT
constexpr size_t WS_XBUF = 0, XBUF_BYTES = (size_t)32 * 8 * 8 * 4096;
constexpr size_t WS_FLAG = XBUF_BYTES;
constexpr size_t WS_TOK = WS_FLAG + 256 * 128;

__global__ __launch_bounds__(512, 2) void fused_kernel(const u16* x, const u16* kc, const u16* vc, const u16* wq, const u16* wo,
                                                       char* ws, u16* out) {
  extern __shared__ __attribute__((aligned(16))) char smem[];
  const int tid = threadIdx.x, lane = tid & 63;
  const int wave = __builtin_amdgcn_readfirstlane(tid >> 6);
  const int hkv = blockIdx.x & 7, b = blockIdx.x >> 3;
  const int w4 = wave & 3;
  const bool isW = wave >= 4;
  const int r16 = lane & 15, g = lane >> 4;
  const int r32 = lane & 31, h = lane >> 5;
  const char* ximg = smem + LDS_X;
  const int stag = b & 3;
  const int rot = b & 15;
  unsigned tok = 0;
  const __amdgpu_buffer_rsrc_t xr = __builtin_amdgcn_make_buffer_rsrc(ws + WS_XBUF, 0, (int)XBUF_BYTES, 0x00020000);
  const unsigned sbase = (unsigned)(size_t)smem;
  const u16* kp = kc + (size_t)(b * HKV + hkv) * SK * DH;
  const u16* vp = vc + (size_t)(b * HKV + hkv) * SK * DH;
#define K_ONE(t) do { \
    int ln_ = lane; asm volatile("" : "+v"(ln_)); \
    const int r_ = ln_ & 15, g_ = ln_ >> 4; \
    _Pragma("unroll") for (int j = 0; j < 4; ++j) { const int row = 16 * w4 + 4 * j + g_; \
      glds16_nt(kp + (t) * 64 * DH, (row * DH + 8 * (r_ ^ (row & 15))) * 2, sbase + LDS_RING + ((t) & 3) * 32768 + (16 * w4 + 4 * j) * 256); } \
  } while (0)
#define V_ONE(t) do { \
    int ln_ = lane; asm volatile("" : "+v"(ln_)); \
    const int r_ = ln_ & 15, g_ = ln_ >> 4; \
    _Pragma("unroll") for (int j = 0; j < 4; ++j) { const int row = 16 * w4 + 4 * j + g_; \
      glds16_nt(vp + (t) * 64 * DH, (row * DH + 8 * (r_ ^ (((row & 3) << 2) | ((row >> 2) & 3)))) * 2, sbase + LDS_RING + ((t) & 3) * 32768 + 16384 + (16 * w4 + 4 * j) * 256); } \
  } while (0)
#define KP(j) do { K_ONE(2 * (j)); K_ONE(2 * (j) + 1); } while (0)
#define VP(j) do { V_ONE(2 * (j)); V_ONE(2 * (j) + 1); } while (0)

  if (!isW) {
    {
      const u16* xp = x + (size_t)b * SQ * DM;
#pragma unroll
      for (int i = 0; i < 8; ++i) {
        const int row = 8 * w4 + i;
        const int c = (lane & ~15) | ((lane & 15) ^ (row & 15));
        glds16(xp, (unsigned)(row * DM + c * 8) * 2, sbase + LDS_X + row * 1024);
      }
    }
    bf16x8 qf[8];
    {
      const u16* wp = wq + hkv * 512 + w4 * 128;
      const unsigned wv = (unsigned)(8 * g * 4096 + r16 * 8) * 2;
      u32x4 raw[4][8];
      wload<4096>(raw[0], wp, wv, (0 + rot) & 15);
      f32x4 acc[8][2];
#pragma unroll
      for (int t = 0; t < 8; ++t) { acc[t][0] = f32x4{0, 0, 0, 0}; acc[t][1] = f32x4{0, 0, 0, 0}; }
      wait_vm<8>();
      barrier_lgkm();
      wload<4096>(raw[1], wp, wv, (1 + rot) & 15);
      wload<4096>(raw[2], wp, wv, (2 + rot) & 15);
      proj_loop<4096, true>(raw, acc, wp, wv, ximg, rot, r16, g, stag);
#pragma unroll
      for (int i = 0; i < 4; ++i) {
        u32x4 a, bb;
#pragma unroll
        for (int m = 0; m < 4; ++m) {
          unsigned a_ = cvtpk(acc[2 * m][0][i], acc[2 * m + 1][0][i]);
          unsigned b_ = cvtpk(acc[2 * m][1][i], acc[2 * m + 1][1][i]);
          auto r = __builtin_amdgcn_permlane16_swap(a_, b_, false, false);
          a[m] = r[0]; bb[m] = r[1];
        }
        qf[i] = __builtin_bit_cast(bf16x8, a);
        qf[4 + i] = __builtin_bit_cast(bf16x8, bb);
      }
    }
    unsigned kaddr[8], vaddr[8];
#pragma unroll
    for (int s = 0; s < 8; ++s) kaddr[s] = r32 * 256 + 16 * ((8 * h + s) ^ (r32 & 15));
    {
      const int blk = (lane >> 4) & 1, qq = (lane & 15) >> 2, p = lane & 3;
#pragma unroll
      for (int dt = 0; dt < 4; ++dt)
#pragma unroll
        for (int e = 0; e < 2; ++e)
          vaddr[dt * 2 + e] = (8 * e + 4 * h + qq) * 256 + 16 * (4 * (dt ^ qq) + ((2 * blk + (p >> 1)) ^ (2 * e + h))) + 8 * (p & 1) + 16384;
    }
    const float csc = 0.08838834764831845f * 1.4426950408889634f;
    float m_run = -__builtin_inff(), l_run = 0.f;
    f32x16 o[4];
#pragma unroll
    for (int dt = 0; dt < 4; ++dt)
#pragma unroll
      for (int i = 0; i < 16; ++i) o[dt][i] = 0.f;

    f32x16 lacc;
#pragma unroll
    for (int i = 0; i < 16; ++i) lacc[i] = 0.f;
    bf16x8 ones;
#pragma unroll
    for (int i = 0; i < 8; ++i) ones[i] = (short)0x3F80;
#pragma unroll 1
    for (int j = 0; j < 4; ++j) {
      barrier_lgkm();
      const char* slot = smem + LDS_RING + (j & 1) * 65536;
      f32x16 sc[4];
      bf16x8 kf[2][4];
      float mxa = m_run, mxb = m_run;
#define K_LOAD(gi, buf) do { _Pragma("unroll") for (int s_ = 0; s_ < 4; ++s_) \
        kf[buf][s_] = *(const bf16x8*)(slot + kaddr[2 * ((gi) & 3) + (s_ >> 1)] + (s_ & 1) * 8192 + ((gi) >> 2) * 32768); } while (0)
      K_LOAD(0, 0);
#pragma unroll
      for (int gi = 0; gi < 8; ++gi) {
        const int pr = gi >> 2;
        __builtin_amdgcn_sched_barrier(0);
        if (gi < 7) K_LOAD(gi + 1, (gi + 1) & 1);
        if ((gi & 3) == 0) {
#pragma unroll
          for (int i = 0; i < 16; ++i) { sc[2 * pr][i] = 0.f; sc[2 * pr + 1][i] = 0.f; }
        }
#pragma unroll
        for (int s = 0; s < 4; ++s)
          sc[2 * pr + (s & 1)] = __builtin_amdgcn_mfma_f32_32x32x16_bf16(kf[gi & 1][s], qf[2 * (gi & 3) + (s >> 1)], sc[2 * pr + (s & 1)], 0, 0, 0);
        if (gi == 5 || gi == 6) {
#pragma unroll
          for (int i = 0; i < 8; ++i) { mxa = fmaxf(mxa, sc[gi - 5][i]); mxb = fmaxf(mxb, sc[gi - 5][8 + i]); }
        }
      }
      bf16x8 pf[2], vf[2][4];
#define V_LOAD(ks, buf) do { \
        const char* vb_ = slot + ((ks) >> 2) * 32768 + 4096 * ((ks) & 3); \
        _Pragma("unroll") for (int dt = 0; dt < 4; ++dt) { \
          const s16x4 lo = __builtin_amdgcn_ds_read_tr16_b64_v4i16((LDS3 s16x4*)(vb_ + vaddr[dt * 2 + 0])); \
          const s16x4 hi = __builtin_amdgcn_ds_read_tr16_b64_v4i16((LDS3 s16x4*)(vb_ + vaddr[dt * 2 + 1])); \
          vf[buf][dt] = __builtin_shufflevector(lo, hi, 0, 1, 2, 3, 4, 5, 6, 7); } } while (0)
#define P_FMA(ks) do { const int u_ = (ks) >> 1, r0_ = 8 * ((ks) & 1); \
        _Pragma("unroll") for (int m = 0; m < 8; ++m) sc[u_][r0_ + m] = fmaf(sc[u_][r0_ + m], csc, nm); } while (0)
#define P_EXP(ks, buf) do { \
        const int u_ = (ks) >> 1, r0_ = 8 * ((ks) & 1); u32x4 pw_; \
        _Pragma("unroll") for (int m = 0; m < 4; ++m) \
          pw_[m] = cvtpk(__builtin_amdgcn_exp2f(sc[u_][r0_ + 2 * m]), __builtin_amdgcn_exp2f(sc[u_][r0_ + 2 * m + 1])); \
        pf[buf] = __builtin_bit_cast(bf16x8, pw_); } while (0)
      barrier_lgkm();
      V_LOAD(0, 0);
#pragma unroll
      for (int i = 0; i < 16; ++i) { mxa = fmaxf(mxa, sc[2][i]); mxb = fmaxf(mxb, sc[3][i]); }
      float mx = fmaxf(mxa, mxb);
      {
        auto rr = __builtin_amdgcn_permlane32_swap(__float_as_uint(mx), __float_as_uint(mx), false, false);
        mx = fmaxf(__uint_as_float(rr[0]), __uint_as_float(rr[1]));
      }
      const float alpha = __builtin_amdgcn_exp2f((m_run - mx) * csc);
      const float nm = -mx * csc;
      m_run = mx;
      P_FMA(0);
      P_FMA(1);
      P_EXP(0, 0);
      lacc[0] *= alpha;
#pragma unroll
      for (int ks = 0; ks < 8; ++ks) {
        __builtin_amdgcn_sched_barrier(0);
        if (ks < 7) { V_LOAD(ks + 1, (ks + 1) & 1); }
        lacc = __builtin_amdgcn_mfma_f32_32x32x16_bf16(ones, pf[ks & 1], lacc, 0, 0, 0);
#pragma unroll
        for (int dt = 0; dt < 4; ++dt) {
          if (ks == 0) {
#pragma unroll
            for (int i = 0; i < 16; ++i) o[dt][i] *= alpha;
          }
          o[dt] = __builtin_amdgcn_mfma_f32_32x32x16_bf16(vf[ks & 1][dt], pf[ks & 1], o[dt], 0, 0, 0);
        }
        if (ks < 7) { P_EXP(ks + 1, (ks + 1) & 1); }
        if (ks < 6) { P_FMA(ks + 2); }
        if (ks > 0 && ks < 6) {
#pragma unroll
          for (int gq = 0; gq < 4; ++gq) {
            __builtin_amdgcn_sched_group_barrier(0x008, 1, 0);
            __builtin_amdgcn_sched_group_barrier(0x100, 2, 0);
            __builtin_amdgcn_sched_group_barrier(0x400, 2, 0);
            __builtin_amdgcn_sched_group_barrier(0x002, 2, 0);
          }
          __builtin_amdgcn_sched_group_barrier(0x008, 1, 0);
          __builtin_amdgcn_sched_group_barrier(0x002, 4, 0);
        }
      }
    }
    l_run = lacc[0];
    const float inv = 1.0f / l_run;
#pragma unroll
    for (int dt = 0; dt < 4; ++dt)
#pragma unroll
      for (int a = 0; a < 4; ++a) {
        u32x2 v;
        v[0] = cvtpk(o[dt][4 * a] * inv, o[dt][4 * a + 1] * inv);
        v[1] = cvtpk(o[dt][4 * a + 2] * inv, o[dt][4 * a + 3] * inv);
        const int c = 16 * w4 + 4 * dt + a;
        *(u32x2*)(smem + LDS_OIMG + (r32 & 15) * 1024 + (r32 >> 4) * 32768 + 16 * (c ^ (r32 & 15)) + 8 * h) = v;
      }
    barrier_lgkm();
    if (wave == 0) {
      gu32* tokp = (gu32*)(ws + WS_TOK + (size_t)b * 128) + (lane & 7);
      gu32* inbox = (gu32*)(ws + WS_FLAG + (size_t)(b * 8 + hkv) * 128) + (lane & 7);
      gu32* outbox = (gu32*)(ws + WS_FLAG + (size_t)(b * 8 + (lane & 7)) * 128) + hkv;
      tok = __hip_atomic_load(tokp, RLX_AGENT);
      const unsigned mytok = __builtin_amdgcn_readlane(tok, hkv);
      barrier_lgkm();
      if (lane < 8) __hip_atomic_store(outbox, tok, RLX_AGENT);
      for (unsigned spin = 0; spin < (1u << 20); ++spin) {
        const unsigned f = __hip_atomic_load(inbox, RLX_AGENT);
        const unsigned t2 = __hip_atomic_load(tokp, RLX_AGENT);
        if (lane < 8 && t2 != tok) { tok = t2; __hip_atomic_store(outbox, tok, RLX_AGENT); }
        if (__all(f == mytok)) break;
        __builtin_amdgcn_s_sleep(1);
      }
      asm volatile("" ::: "memory");
    } else {
      barrier_lgkm();
    }
  } else {
    barrier_lgkm();
    if (wave == 4) {
      gu32* inbox = (gu32*)(ws + WS_FLAG + (size_t)(b * 8 + hkv) * 128);
      gu32* mytokp = (gu32*)(ws + WS_TOK + (size_t)b * 128) + hkv;
      const unsigned v = __hip_atomic_load(lane < 8 ? inbox + lane : mytokp, RLX_AGENT);
      unsigned t = __builtin_amdgcn_readlane(v, 8) + 1;
      while (__any(v == t)) ++t;
      if (lane == 0) __hip_atomic_store(mytokp, t, RLX_AGENT);
    }
    const u16* wp = wo + (size_t)(hkv * 512) * 512 + w4 * 128;
    const unsigned wv = (unsigned)(8 * g * 512 + r16 * 8) * 2;
    u32x4 raw[4][8];
    const unsigned lst0 = sbase + LDS_X + w4 * 8192;
    const unsigned lst1 = sbase + LDS_RING + (w4 >> 1) * 32768 + (w4 & 1) * 8192;
    const unsigned lst2 = lst1 + 16384;
#define WO_LDS(idx, dst) do { const u16* q_ = wp + (size_t)(32 * (((idx) + rot) & 15)) * 512; \
      _Pragma("unroll") for (int j = 0; j < 8; ++j) glds16(q_ + j * 512, wv, (dst) + j * 1024); } while (0)
    barrier_raw();
    barrier_raw();
    KP(0);
    barrier_raw();
    VP(0);
    wait_vm<8>();
    barrier_lgkm();
    KP(1);
    VP(1);
    WO_LDS(4, lst0);
    wait_vm<24>();
    barrier_lgkm();
    KP(2);
    wload<512>(raw[0], wp, wv, (0 + rot) & 15);
    wait_vm<32>();
    barrier_lgkm();
    VP(2);
    wload<512>(raw[1], wp, wv, (1 + rot) & 15);
    wait_vm<40>();
    barrier_lgkm();
    KP(3);
    wload<512>(raw[2], wp, wv, (2 + rot) & 15);
    wait_vm<40>();
    barrier_lgkm();
    VP(3);
    wload<512>(raw[3], wp, wv, (3 + rot) & 15);
    wait_vm<40>();
    barrier_lgkm();
    WO_LDS(5, lst1);
    wait_vm<32>();
    barrier_lgkm();
    WO_LDS(6, lst2);
    wait_vm<24>();
    barrier_lgkm();
    f32x4 acc[8][2];
#pragma unroll
    for (int t = 0; t < 8; ++t) { acc[t][0] = f32x4{0, 0, 0, 0}; acc[t][1] = f32x4{0, 0, 0, 0}; }
    wait_raw<0>(raw[0]); wait_raw<0>(raw[1]); wait_raw<0>(raw[2]); wait_raw<0>(raw[3]);
    barrier_lgkm();
    {
      const char* oimg = smem + LDS_OIMG;
      bf16x8 bf[2];
      bload<32768>(bf, oimg, rot & 15, r16, g);
#pragma unroll
      for (int c = 0; c < 4; ++c) {
        wstep<32768>(raw[c], acc, oimg, bf, (c + 1 + rot) & 15, r16, g);
        __builtin_amdgcn_sched_barrier(0);
        wload<512>(raw[c], wp, wv, (7 + c + rot) & 15);
      }
#pragma unroll
      for (int c = 0; c < 3; ++c) {
        const char* src = smem + (c == 0 ? LDS_X + w4 * 8192 : LDS_RING + (w4 >> 1) * 32768 + (w4 & 1) * 8192 + (c == 2 ? 16384 : 0)) + lane * 16;
        u32x4 tmp[8];
#pragma unroll
        for (int j = 0; j < 8; ++j) tmp[j] = *(const u32x4*)(src + j * 1024);
        wstep<32768>(tmp, acc, oimg, bf, (5 + c + rot) & 15, r16, g);
        __builtin_amdgcn_sched_barrier(0);
      }
#pragma unroll
      for (int c = 7; c < 16; ++c) {
        const int bi = (c - 7) & 3;
        if (c <= 12) wait_raw<24>(raw[bi]); else if (c == 13) wait_raw<16>(raw[bi]); else if (c == 14) wait_raw<8>(raw[bi]); else wait_raw<0>(raw[bi]);
        wstep<32768>(raw[bi], acc, oimg, bf, (c + 1 + rot) & 15, r16, g);
        __builtin_amdgcn_sched_barrier(0);
        if (c + 4 < 16) wload<512>(raw[bi], wp, wv, (c + 4 + rot) & 15);
      }
    }
    {
      const unsigned off = (unsigned)(((b * 8 + 2 * w4 + (g >> 1)) * 8 + hkv) * 4096 + (lane & 31) * 16);
#pragma unroll
      for (int qt = 0; qt < 2; ++qt)
#pragma unroll
        for (int i = 0; i < 4; ++i) {
          u32x4 v;
#pragma unroll
          for (int m = 0; m < 4; ++m) v[m] = cvtpk(acc[2 * m][qt][i], acc[2 * m + 1][qt][i]);
          __builtin_amdgcn_raw_buffer_store_b128(v, xr, off + (qt * 4 + i) * 512, 0, 16);
        }
    }
    wait_vm<0>();
    barrier_lgkm();
  }
  barrier_lgkm();
  {
    const unsigned off = (unsigned)(((b * 8 + hkv) * 8 + (lane >> 5)) * 4096 + wave * 512 + (lane & 31) * 16);
    u32x4 d[4];
#pragma unroll
    for (int p = 0; p < 4; ++p) d[p] = __builtin_amdgcn_raw_buffer_load_b128(xr, off + p * 8192, 0, 16);
    float sum[8];
#pragma unroll
    for (int m = 0; m < 4; ++m) {
      float lo = 0.f, hi = 0.f;
#pragma unroll
      for (int p = 0; p < 4; ++p) { lo += __uint_as_float(d[p][m] << 16); hi += __uint_as_float(d[p][m] & 0xffff0000u); }
      sum[2 * m] = lo; sum[2 * m + 1] = hi;
    }
#pragma unroll
    for (int e = 0; e < 8; ++e) {
      auto rr = __builtin_amdgcn_permlane32_swap(__float_as_uint(sum[e]), __float_as_uint(sum[e]), false, false);
      sum[e] = __uint_as_float(rr[0]) + __uint_as_float(rr[1]);
    }
    if (lane < 32) {
      u32x4 v;
#pragma unroll
      for (int m = 0; m < 4; ++m) v[m] = cvtpk(sum[2 * m], sum[2 * m + 1]);
      const int q = (lane & 15) + 16 * (wave >> 2), n = 64 * hkv + 32 * ((lane >> 4) & 1) + 8 * (wave & 3);
      *(u32x4*)(out + (size_t)b * (SQ * DM) + q * DM + n) = v;
    }
  }
  if (wave == 0) {
    gu32* tokp = (gu32*)(ws + WS_TOK + (size_t)b * 128) + (lane & 7);
    gu32* outbox = (gu32*)(ws + WS_FLAG + (size_t)(b * 8 + (lane & 7)) * 128) + hkv;
    const unsigned t2 = __hip_atomic_load(tokp, RLX_AGENT);
    if (lane < 8 && t2 != tok) __hip_atomic_store(outbox, t2, RLX_AGENT);
  }
}

extern "C" void kernel_launch(void* const* d_in, const int* in_sizes, int n_in, void* d_out, int out_size, void* d_ws,
                              size_t ws_size, hipStream_t stream) {
  const u16* x = (const u16*)d_in[0];
  const u16* kc = (const u16*)d_in[1];
  const u16* vc = (const u16*)d_in[2];
  const u16* wq = (const u16*)d_in[3];
  const u16* wo = (const u16*)d_in[4];
  (void)hipFuncSetAttribute((const void*)fused_kernel, hipFuncAttributeMaxDynamicSharedMemorySize, LDS_TOTAL);
  fused_kernel<<<256, 512, LDS_TOTAL, stream>>>(x, kc, vc, wq, wo, (char*)d_ws, (u16*)d_out);
}
```
